```python
import torch

def _softmax(x, axis=-1):
    return torch.softmax(x, dim=axis)

def _gelu(x):
    return torch.nn.functional.gelu(x, approximate='tanh')

def _silu(x):
    return torch.nn.functional.silu(x)

def _sigmoid(x):
    return torch.sigmoid(x)

def _layernorm(x, axis=-1, eps=1e-05):
    m = x.mean(dim=axis, keepdim=True)
    v = x.var(dim=axis, keepdim=True, unbiased=False)
    return (x - m) / torch.sqrt(v + eps)

def _scatter_add(tgt, idx, src, axis=0):
    return tgt.clone().index_add_(axis, idx, src)

def _segment_sum(src, seg, n):
    out = torch.zeros((n,) + src.shape[1:], dtype=src.dtype)
    return out.index_add_(0, seg, src)

def _pad(x, pw):
    flat = tuple((v for pair in reversed(pw) for v in pair))
    return torch.nn.functional.pad(x, flat)
kernel_name = 'interleave_layers__linear_kernel_hedgehog__mixer_gmlp__prompt'

def setup_inputs():

    _w = lambda a: (lambda t: t.to(torch.bfloat16) if t.is_floating_point() else t)(torch.as_tensor(a))

    def _A_setup():
        B, S, D, H = (1, 2048, 640, 32)
        Dh = D // H
        return {'x': _w((lambda *s: torch.randn(*s, dtype=torch.bfloat16) * 0.05)(B, S, D)), 'wqkv': _w((lambda *s: torch.randn(*s, dtype=torch.bfloat16) * 0.4)(D, 3 * D)), 'w_phi': _w((lambda *s: torch.randn(*s, dtype=torch.bfloat16) * 0.4)(H, Dh, Dh) * Dh ** (-0.5)), 'wo': _w((lambda *s: torch.randn(*s, dtype=torch.bfloat16) * 0.05)(D, D))}
    _A_in = _A_setup()

    def _B_setup():
        B, S, D, F = (1, 2048, 640, 1536)
        Fh = F // 2
        return {'x': _w((lambda *s: torch.randn(*s, dtype=torch.bfloat16) * 0.05)(B, S, D)), 'w_in': _w((lambda *s: torch.randn(*s, dtype=torch.bfloat16) * 0.4)(D, 2 * Fh)), 'w_sp': _w(0.01 * (lambda *s: torch.randn(*s, dtype=torch.bfloat16) * 0.4)(S, S)), 'b_sp': _w(1.0 + 0.01 * (lambda *s: torch.randn(*s, dtype=torch.bfloat16) * 0.05)(S)), 'w_out': _w((lambda *s: torch.randn(*s, dtype=torch.bfloat16) * 0.05)(Fh, D))}
    _B_in = _B_setup()
    return {'A_x': _A_in['x'], 'A_wqkv': _A_in['wqkv'], 'A_w_phi': _A_in['w_phi'], 'A_wo': _A_in['wo'], 'B_x': _B_in['x'], 'B_w_in': _B_in['w_in'], 'B_w_sp': _B_in['w_sp'], 'B_b_sp': _B_in['b_sp'], 'B_w_out': _B_in['w_out']}

def reference(A_x, A_wqkv, A_w_phi, A_wo, B_x, B_w_in, B_w_sp, B_b_sp, B_w_out):

    def _A_ref(x, wqkv, w_phi, wo):
        B, S, D = x.shape
        H = 32
        Dh = D // H
        qkv = (lambda x, *d: x.permute(*d))((x @ wqkv).reshape(B, S, 3, H, Dh), 2, 0, 3, 1, 4)
        q, k, v = (qkv[0], qkv[1], qkv[2])
        pq = torch.einsum('bhsd,hdf->bhsf', q, w_phi)
        pk = torch.einsum('bhsd,hdf->bhsf', k, w_phi)
        phi_q = torch.concatenate([_softmax(pq, axis=-1), _softmax(-pq, axis=-1)], axis=-1)
        phi_k = torch.concatenate([_softmax(pk, axis=-1), _softmax(-pk, axis=-1)], axis=-1)
        kv = torch.einsum('bhsf,bhsd->bhfd', phi_k, v)
        z = torch.einsum('bhsf->bhf', phi_k)
        num = torch.einsum('bhqf,bhfd->bhqd', phi_q, kv)
        den = torch.einsum('bhqf,bhf->bhq', phi_q, z)[..., None] + 1e-06
        o = num / den
        return (lambda x, *d: x.permute(*d))(o, 0, 2, 1, 3).reshape(B, S, D) @ wo

    def _B_ref(x, w_in, w_sp, b_sp, w_out):
        B, S, D = x.shape
        F = w_in.shape[-1]
        Fh = F // 2
        z = _gelu(torch.einsum('bsd,df->bsf', x, w_in))
        z1 = z[..., :Fh].clone()
        z2 = _layernorm(z[..., Fh:], axis=-1)
        g = torch.einsum('ts,bsf->btf', w_sp, z2) + b_sp[None, :, None]
        return torch.einsum('bsf,fd->bsd', z1 * g, w_out)
    x = A_x
    x = x + _A_ref(_layernorm(x, axis=-1), A_wqkv, A_w_phi, A_wo)
    x = x + _B_ref(_layernorm(x, axis=-1), B_w_in, B_w_sp, B_b_sp, B_w_out)
    x = x + _A_ref(_layernorm(x, axis=-1), A_wqkv, A_w_phi, A_wo)
    x = x + _B_ref(_layernorm(x, axis=-1), B_w_in, B_w_sp, B_b_sp, B_w_out)
    return x
```

```cpp
#include <hip/hip_runtime.h>
#include <stdint.h>

typedef unsigned short u16;
typedef __attribute__((ext_vector_type(8))) short s16x8;
typedef __attribute__((ext_vector_type(4))) short s16x4;
typedef __attribute__((ext_vector_type(4))) float f32x4;
typedef __attribute__((address_space(3))) s16x4* lds_s16x4_ptr;

constexpr int S_ = 2048, D_ = 640, H_ = 32, DH_ = 20, D3_ = 1920, FH_ = 768, F2_ = 1536;

__device__ __forceinline__ float bf2f(u16 v) { return __uint_as_float(((unsigned)v) << 16); }
__device__ __forceinline__ u16 f2bf(float f) { return __builtin_bit_cast(u16, (__bf16)f); }
__device__ __forceinline__ float rbf(float f) { return bf2f(f2bf(f)); }
__device__ __forceinline__ unsigned pack2(float a, float b) { return (unsigned)f2bf(a) | ((unsigned)f2bf(b) << 16); }

__device__ __forceinline__ f32x4 mfma16(s16x8 a, s16x8 b, f32x4 c) {
  return __builtin_amdgcn_mfma_f32_16x16x32_bf16(a, b, c, 0, 0, 0);
}
__device__ __forceinline__ s16x4 tr_read(const char* p) {
  return __builtin_amdgcn_ds_read_tr16_b64_v4i16((lds_s16x4_ptr)(uintptr_t)(unsigned)(uintptr_t)p);
}
__device__ __forceinline__ s16x8 cat8(s16x4 a, s16x4 b) {
  s16x8 r;
  r[0] = a[0]; r[1] = a[1]; r[2] = a[2]; r[3] = a[3];
  r[4] = b[0]; r[5] = b[1]; r[6] = b[2]; r[7] = b[3];
  return r;
}

__device__ __forceinline__ void ln_finish(float mean, float var, float& m_b, float& inv) {
  m_b = rbf(mean);
  float v_b = rbf(var);
  float den = rbf(sqrtf(rbf(v_b + 1e-5f)));
  inv = 1.0f / den;
}
__device__ __forceinline__ float ln_apply(float x, float m_b, float inv) { return rbf(x - m_b) * inv; }

template <int CNT>
__device__ __forceinline__ void ln_combine8(float2 part, float& m_b, float& inv) {
  float s = part.x;
  s += __shfl_xor(s, 1); s += __shfl_xor(s, 2); s += __shfl_xor(s, 4);
  float mu = s * 0.125f;
  float dm = part.x - mu;
  float m2 = part.y + (float)CNT * dm * dm;
  m2 += __shfl_xor(m2, 1); m2 += __shfl_xor(m2, 2); m2 += __shfl_xor(m2, 4);
  ln_finish(mu, m2 * (1.0f / (8 * CNT)), m_b, inv);
}

__device__ __forceinline__ uint4 ln_chunk(uint4 raw, float m_b, float inv) {
  uint4 r;
  unsigned* s = (unsigned*)&raw;
  unsigned* d = (unsigned*)&r;
#pragma unroll
  for (int i = 0; i < 4; ++i) {
    float lo = __uint_as_float(s[i] << 16), hi = __uint_as_float(s[i] & 0xffff0000u);
    d[i] = pack2(ln_apply(lo, m_b, inv), ln_apply(hi, m_b, inv));
  }
  return r;
}

constexpr int PSTR = 64 * 64 + 64;
template <int BM, int BN>
struct GemmCfg {
  static constexpr int NPANEL = (BN + 31) / 32;
  static constexpr int A_BYTES = BM * 128;
  static constexpr int B_BYTES = NPANEL * PSTR;
  static constexpr int STAGE = A_BYTES + B_BYTES;
  static constexpr int A_PER = BM * 8 / 256;
  static constexpr int CPR = BN / 8;
  static constexpr int B_TOT = 64 * CPR;
  static constexpr int B_PER = (B_TOT + 255) / 256;
};

template <int BM, int BN, int WGM, int WGN, int KTOT, class ALd, class AXf, class BLd, class BXf>
__device__ __forceinline__ void mainloop(char* smem, ALd ald, AXf axf, BLd bld, BXf bxf,
                                         f32x4 (&acc)[BM / WGM / 16][BN / WGN / 16]) {
  using C = GemmCfg<BM, BN>;
  constexpr int MT = BM / WGM / 16, NT = BN / WGN / 16, NIT = KTOT / 64;
  const int tid = threadIdx.x, lane = tid & 63, wid = tid >> 6;
  const int wm = wid / WGN, wn = wid % WGN;
  const int g = lane >> 4, c16 = lane & 15;
  uint4 ra[C::A_PER], rb[C::B_PER];

  auto gload = [&](int it) {
#pragma unroll
    for (int i = 0; i < C::A_PER; ++i) ra[i] = ald(it, i);
#pragma unroll
    for (int i = 0; i < C::B_PER; ++i) {
      if (C::B_TOT % 256 == 0 || tid + 256 * i < C::B_TOT) rb[i] = bld(it, i);
    }
  };
  auto sstore = [&](int it, char* buf) {
#pragma unroll
    for (int i = 0; i < C::A_PER; ++i) {
      int id = tid + 256 * i, row = id >> 3, c = id & 7;
      *(uint4*)(buf + row * 128 + ((c ^ (row & 7)) << 4)) = axf(ra[i], it, i);
    }
#pragma unroll
    for (int i = 0; i < C::B_PER; ++i) {
      int id = tid + 256 * i;
      if (C::B_TOT % 256 == 0 || id < C::B_TOT) {
        int kr = id / C::CPR, c = id % C::CPR;
        *(uint4*)(buf + C::A_BYTES + (c >> 2) * PSTR + kr * 64 + (((c & 3) << 4) ^ (((kr >> 3) & 1) << 5))) =
            bxf(rb[i], it, i);
      }
    }
  };

#pragma unroll
  for (int m = 0; m < MT; ++m)
#pragma unroll
    for (int n = 0; n < NT; ++n) acc[m][n] = f32x4{0.f, 0.f, 0.f, 0.f};

  gload(0);
  sstore(0, smem);
  __syncthreads();

  const int a_off = (wm * MT * 16 + c16) * 128;
  const int q = c16 >> 2, p4 = lane & 3;
  const int b_off = C::A_BYTES + (8 * g + q) * 64 + p4 * 8;

  for (int it = 0; it < NIT; ++it) {
    char* buf = smem + (it & 1) * C::STAGE;
    if (it + 1 < NIT) gload(it + 1);
#pragma unroll
    for (int ks = 0; ks < 2; ++ks) {
      s16x8 af[MT], bfr[NT];
#pragma unroll
      for (int m = 0; m < MT; ++m)
        af[m] = *(const s16x8*)(buf + a_off + m * 16 * 128 + (((4 * ks + g) ^ (c16 & 7)) << 4));
#pragma unroll
      for (int n = 0; n < NT; ++n) {
        int nb = wn * NT + n;
        const char* p = buf + b_off + (nb >> 1) * PSTR + ks * 32 * 64 + (((nb & 1) ^ (g & 1)) << 5);
        bfr[n] = cat8(tr_read(p), tr_read(p + 4 * 64));
      }
#pragma unroll
      for (int m = 0; m < MT; ++m)
#pragma unroll
        for (int n = 0; n < NT; ++n) acc[m][n] = mfma16(af[m], bfr[n], acc[m][n]);
    }
    if (it + 1 < NIT) sstore(it + 1, smem + ((it + 1) & 1) * C::STAGE);
    __syncthreads();
  }
}

__global__ __launch_bounds__(256) void k_stats0(const u16* __restrict__ x, float2* __restrict__ st) {
  int row = blockIdx.x * 4 + (threadIdx.x >> 6), lane = threadIdx.x & 63;
  const unsigned* p = (const unsigned*)(x + (size_t)row * D_ + lane * 10);
  float v[10];
#pragma unroll
  for (int i = 0; i < 5; ++i) {
    unsigned w = p[i];
    v[2 * i] = __uint_as_float(w << 16);
    v[2 * i + 1] = __uint_as_float(w & 0xffff0000u);
  }
  float s = 0.f;
#pragma unroll
  for (int i = 0; i < 10; ++i) s += v[i];
  s += __shfl_xor(s, 1); s += __shfl_xor(s, 2); s += __shfl_xor(s, 4);
  float mu = s * (1.0f / 80.0f), m2 = 0.f;
#pragma unroll
  for (int i = 0; i < 10; ++i) m2 += (v[i] - mu) * (v[i] - mu);
  m2 += __shfl_xor(m2, 1); m2 += __shfl_xor(m2, 2); m2 += __shfl_xor(m2, 4);
  if ((lane & 7) == 0) st[row * 8 + (lane >> 3)] = make_float2(mu, m2);
}

constexpr int TSTR = 176;
constexpr int PSTRIDE = 168;
constexpr int KVP = 24;
__global__ __launch_bounds__(256) void k_qkv(const u16* __restrict__ x, const float2* __restrict__ st,
                                             const u16* __restrict__ wqkv, const u16* __restrict__ wphi,
                                             u16* __restrict__ phiq, float* __restrict__ kvpart) {
  extern __shared__ __attribute__((aligned(16))) char smem[];
  constexpr int BM = 128, BN = 160;
  using C = GemmCfg<BM, BN>;
  const int tid = threadIdx.x, lane = tid & 63, wid = tid >> 6;
  const int rt = blockIdx.x / 12, ct = blockIdx.x % 12;
  const bool isq = ct < 4;
  const int row0 = rt * BM;

  float m_b[4], inv[4];
#pragma unroll
  for (int i = 0; i < 4; ++i) {
    int r = row0 + (tid >> 3) + 32 * i;
    ln_combine8<80>(st[r * 8 + (tid & 7)], m_b[i], inv[i]);
  }
  int boff[C::B_PER];
#pragma unroll
  for (int i = 0; i < C::B_PER; ++i) {
    int id = tid + 256 * i, kr = id / 20, c = id % 20;
    int col = isq ? ct * 160 + c * 8 : (c < 10 ? D_ + (ct - 4) * 80 + c * 8 : 2 * D_ + (ct - 4) * 80 + (c - 10) * 8);
    boff[i] = kr * D3_ + col;
  }
  const u16* ap = x + (size_t)(row0 + (tid >> 3)) * D_ + (tid & 7) * 8;
  auto ald = [&](int it, int i) { return *(const uint4*)(ap + (size_t)i * 32 * D_ + it * 64); };
  auto axf = [&](uint4 r, int it, int i) { return ln_chunk(r, m_b[i], inv[i]); };
  auto bld = [&](int it, int i) { return *(const uint4*)(wqkv + (size_t)it * 64 * D3_ + boff[i]); };
  auto bxf = [&](uint4 r, int it, int i) { return r; };

  const int g = lane >> 4, c16 = lane & 15;
  const int nh = isq ? 2 : 1;
  s16x8 wA[2][2];
#pragma unroll
  for (int jj = 0; jj < 2; ++jj) {
    int j = isq ? 2 * wid + jj : wid;
    int h = isq ? ct * 8 + j : (ct - 4) * 4 + j;
#pragma unroll
    for (int ft = 0; ft < 2; ++ft) {
      int f = 16 * ft + c16;
#pragma unroll
      for (int e = 0; e < 8; ++e) {
        int d = 8 * g + e;
        wA[jj][ft][e] = (f < DH_ && d < DH_ && jj < nh) ? (short)wphi[(h * DH_ + d) * DH_ + f] : (short)0;
      }
    }
  }

  f32x4 acc[4][5];
  mainloop<BM, BN, 2, 2, D_>(smem, ald, axf, bld, bxf, acc);

  u16* T = (u16*)smem;
  u16* P = (u16*)(smem + 128 * TSTR * 2);
  {
    const int wm = wid >> 1, wn = wid & 1;
#pragma unroll
    for (int m = 0; m < 4; ++m)
#pragma unroll
      for (int n = 0; n < 5; ++n)
#pragma unroll
        for (int i = 0; i < 4; ++i)
          T[(wm * 64 + m * 16 + 4 * g + i) * TSTR + wn * 80 + n * 16 + c16] = f2bf(acc[m][n][i]);
  }
  __syncthreads();
  for (int jj = 0; jj < nh; ++jj) {
    int j = isq ? 2 * wid + jj : wid;
    int h = isq ? ct * 8 + j : (ct - 4) * 4 + j;
#pragma unroll 1
    for (int mt = 0; mt < 8; ++mt) {
      const u16* src = T + (mt * 16 + c16) * TSTR + 20 * j + 8 * g;
      s16x4 lo = *(const s16x4*)src, hi = *(const s16x4*)(src + 4);
      s16x8 bfr = cat8(lo, hi);
      f32x4 z4 = f32x4{0.f, 0.f, 0.f, 0.f};
      f32x4 p0 = mfma16(wA[jj][0], bfr, z4);
      f32x4 p1 = mfma16(wA[jj][1], bfr, z4);
      float a[8];
#pragma unroll
      for (int i = 0; i < 4; ++i) { a[i] = rbf(p0[i]); a[4 + i] = rbf(p1[i]); }
      const bool v1 = (g == 0);
      float mx = fmaxf(fmaxf(a[0], a[1]), fmaxf(a[2], a[3]));
      float mn = fminf(fminf(a[0], a[1]), fminf(a[2], a[3]));
      if (v1) {
        mx = fmaxf(mx, fmaxf(fmaxf(a[4], a[5]), fmaxf(a[6], a[7])));
        mn = fminf(mn, fminf(fminf(a[4], a[5]), fminf(a[6], a[7])));
      }
      mx = fmaxf(mx, __shfl_xor(mx, 16)); mx = fmaxf(mx, __shfl_xor(mx, 32));
      mn = fminf(mn, __shfl_xor(mn, 16)); mn = fminf(mn, __shfl_xor(mn, 32));
      float e1[8], e2[8], s1 = 0.f, s2 = 0.f;
#pragma unroll
      for (int i = 0; i < 8; ++i) {
        e1[i] = __expf(a[i] - mx);
        e2[i] = __expf(mn - a[i]);
        if (i < 4 || v1) { s1 += e1[i]; s2 += e2[i]; }
      }
      s1 += __shfl_xor(s1, 16); s1 += __shfl_xor(s1, 32);
      s2 += __shfl_xor(s2, 16); s2 += __shfl_xor(s2, 32);
      float r1 = 1.0f / s1, r2 = 1.0f / s2;
      uint2 o10 = make_uint2(pack2(e1[0] * r1, e1[1] * r1), pack2(e1[2] * r1, e1[3] * r1));
      uint2 o20 = make_uint2(pack2(e2[0] * r2, e2[1] * r2), pack2(e2[2] * r2, e2[3] * r2));
      uint2 o11 = make_uint2(pack2(e1[4] * r1, e1[5] * r1), pack2(e1[6] * r1, e1[7] * r1));
      uint2 o21 = make_uint2(pack2(e2[4] * r2, e2[5] * r2), pack2(e2[6] * r2, e2[7] * r2));
      if (isq) {
        u16* dst = phiq + (size_t)(row0 + mt * 16 + c16) * 1280 + h * 40;
        *(uint2*)(dst + 4 * g) = o10;
        *(uint2*)(dst + 20 + 4 * g) = o20;
        if (v1) { *(uint2*)(dst + 16) = o11; *(uint2*)(dst + 36) = o21; }
      } else {
        u16* dst = P + (mt * 16 + c16) * PSTRIDE + 40 * j;
        *(uint2*)(dst + 4 * g) = o10;
        *(uint2*)(dst + 20 + 4 * g) = o20;
        if (v1) { *(uint2*)(dst + 16) = o11; *(uint2*)(dst + 36) = o21; }
      }
    }
  }
  if (isq) return;
  __syncthreads();
  {
    const int j = wid, h = (ct - 4) * 4 + j;
    const int q = c16 >> 2, p4 = lane & 3;
    f32x4 kacc[3][2];
#pragma unroll
    for (int a = 0; a < 3; ++a)
#pragma unroll
      for (int b = 0; b < 2; ++b) kacc[a][b] = f32x4{0.f, 0.f, 0.f, 0.f};
    s16x8 ones;
#pragma unroll
    for (int e = 0; e < 8; ++e) ones[e] = (c16 == 4) ? (short)0x3F80 : (short)0;
#pragma unroll
    for (int ks = 0; ks < 4; ++ks) {
      int r0 = 32 * ks + 8 * g + q;
      s16x8 af[3], bfr[2];
#pragma unroll
      for (int ft = 0; ft < 3; ++ft) {
        const char* p = (const char*)(P + r0 * PSTRIDE + 40 * j + 16 * ft + 4 * p4);
        af[ft] = cat8(tr_read(p), tr_read(p + 4 * PSTRIDE * 2));
      }
#pragma unroll
      for (int nt = 0; nt < 2; ++nt) {
        const char* p = (const char*)(T + r0 * TSTR + 80 + 20 * j + 16 * nt + 4 * p4);
        bfr[nt] = cat8(tr_read(p), tr_read(p + 4 * TSTR * 2));
      }
      if (c16 >= 4) bfr[1] = ones;
#pragma unroll
      for (int ft = 0; ft < 3; ++ft)
#pragma unroll
        for (int nt = 0; nt < 2; ++nt) kacc[ft][nt] = mfma16(af[ft], bfr[nt], kacc[ft][nt]);
    }
    float* dst = kvpart + (size_t)(rt * H_ + h) * 40 * KVP;
#pragma unroll
    for (int ft = 0; ft < 3; ++ft)
#pragma unroll
      for (int nt = 0; nt < 2; ++nt)
#pragma unroll
        for (int i = 0; i < 4; ++i) {
          int f = 16 * ft + 4 * g + i, d = 16 * nt + c16;
          if (f < 40 && d <= 20) dst[f * KVP + d] = kacc[ft][nt][i];
        }
  }
}

__global__ __launch_bounds__(256) void k_o(const float* __restrict__ kvpart, const u16* __restrict__ phiq,
                                           u16* __restrict__ o) {
  __shared__ __attribute__((aligned(16))) u16 KV[32 * 64];
  const int tid = threadIdx.x, lane = tid & 63, wid = tid >> 6;
  const int h = blockIdx.x >> 3, tt = blockIdx.x & 7;
  for (int e = tid; e < 32 * 64; e += 256) {
    int d = e >> 6, f = e & 63;
    float s = 0.f;
    if (d <= 20 && f < 40) {
      const float* p = kvpart + (size_t)h * 40 * KVP + f * KVP + d;
#pragma unroll
      for (int r = 0; r < 16; ++r) s += p[(size_t)r * H_ * 40 * KVP];
    }
    KV[e] = f2bf(s);
  }
  __syncthreads();
  const int g = lane >> 4, c16 = lane & 15;
  s16x8 af[2][2];
#pragma unroll
  for (int mt = 0; mt < 2; ++mt)
#pragma unroll
    for (int ks = 0; ks < 2; ++ks) af[mt][ks] = *(const s16x8*)(KV + (16 * mt + c16) * 64 + 32 * ks + 8 * g);
#pragma unroll
  for (int nt = 0; nt < 4; ++nt) {
    int tok = tt * 256 + wid * 64 + nt * 16 + c16;
    const u16* src = phiq + (size_t)tok * 1280 + h * 40 + 8 * g;
    s16x8 b0 = *(const s16x8*)src;
    s16x8 b1;
    if (g == 0) b1 = *(const s16x8*)(src + 32);
    else {
#pragma unroll
      for (int e = 0; e < 8; ++e) b1[e] = 0;
    }
    f32x4 z4 = f32x4{0.f, 0.f, 0.f, 0.f};
    f32x4 c0 = mfma16(af[0][0], b0, z4);
    c0 = mfma16(af[0][1], b1, c0);
    f32x4 c1 = mfma16(af[1][0], b0, z4);
    c1 = mfma16(af[1][1], b1, c1);
    float den = __shfl(c1[0], 16 + c16);
    den = rbf(rbf(den) + 1e-6f);
    u16* dst = o + (size_t)tok * D_ + h * DH_;
    *(uint2*)(dst + 4 * g) = make_uint2(pack2(rbf(c0[0]) / den, rbf(c0[1]) / den), pack2(rbf(c0[2]) / den, rbf(c0[3]) / den));
    if (g == 0)
      *(uint2*)(dst + 16) = make_uint2(pack2(rbf(c1[0]) / den, rbf(c1[1]) / den), pack2(rbf(c1[2]) / den, rbf(c1[3]) / den));
  }
}

template <int KTOT>
__global__ __launch_bounds__(256) void k_res_gemm(const u16* __restrict__ a, const u16* __restrict__ w,
                                                  const u16* __restrict__ xin, u16* __restrict__ xo,
                                                  float2* __restrict__ st) {
  extern __shared__ __attribute__((aligned(16))) char smem[];
  constexpr int BM = 64, BN = 80;
  using C = GemmCfg<BM, BN>;
  const int tid = threadIdx.x, lane = tid & 63, wid = tid >> 6;
  const int rt = blockIdx.x >> 3, ct = blockIdx.x & 7;
  const int row0 = rt * BM, col0 = ct * BN;
  int boff[C::B_PER];
#pragma unroll
  for (int i = 0; i < C::B_PER; ++i) {
    int id = tid + 256 * i, kr = id / C::CPR, c = id % C::CPR;
    boff[i] = kr * D_ + col0 + c * 8;
  }
  const u16* ap = a + (size_t)(row0 + (tid >> 3)) * KTOT + (tid & 7) * 8;
  auto ald = [&](int it, int i) { return *(const uint4*)(ap + (size_t)i * 32 * KTOT + it * 64); };
  auto axf = [&](uint4 r, int it, int i) { return r; };
  auto bld = [&](int it, int i) { return *(const uint4*)(w + (size_t)it * 64 * D_ + boff[i]); };
  auto bxf = [&](uint4 r, int it, int i) { return r; };
  f32x4 acc[1][5];
  mainloop<BM, BN, 4, 1, KTOT>(smem, ald, axf, bld, bxf, acc);
  const int g = lane >> 4, c16 = lane & 15;
  float xv[4][5];
#pragma unroll
  for (int i = 0; i < 4; ++i) {
    size_t base = (size_t)(row0 + wid * 16 + 4 * g + i) * D_ + col0 + c16;
#pragma unroll
    for (int n = 0; n < 5; ++n) {
      float r = rbf(acc[0][n][i]);
      float y = rbf(bf2f(xin[base + 16 * n]) + r);
      xv[i][n] = y;
      xo[base + 16 * n] = f2bf(y);
    }
  }
  if (st != nullptr) {
#pragma unroll
    for (int i = 0; i < 4; ++i) {
      float s = xv[i][0] + xv[i][1] + xv[i][2] + xv[i][3] + xv[i][4];
      s += __shfl_xor(s, 1); s += __shfl_xor(s, 2); s += __shfl_xor(s, 4); s += __shfl_xor(s, 8);
      float mu = s * (1.0f / 80.0f), m2 = 0.f;
#pragma unroll
      for (int n = 0; n < 5; ++n) m2 += (xv[i][n] - mu) * (xv[i][n] - mu);
      m2 += __shfl_xor(m2, 1); m2 += __shfl_xor(m2, 2); m2 += __shfl_xor(m2, 4); m2 += __shfl_xor(m2, 8);
      if (c16 == 0) st[(row0 + wid * 16 + 4 * g + i) * 8 + ct] = make_float2(mu, m2);
    }
  }
}

__device__ __forceinline__ float gelu_tanh(float y) {
  float u = 0.7978845608028654f * (y + 0.044715f * y * y * y);
  float e = __expf(2.0f * u);
  float th = 1.0f - 2.0f / (e + 1.0f);
  return 0.5f * y * (1.0f + th);
}
__global__ __launch_bounds__(256) void k_win(const u16* __restrict__ x, const float2* __restrict__ st,
                                             const u16* __restrict__ win, u16* __restrict__ z1,
                                             u16* __restrict__ y2, float2* __restrict__ yst) {
  extern __shared__ __attribute__((aligned(16))) char smem[];
  constexpr int BM = 128, BN = 96;
  using C = GemmCfg<BM, BN>;
  const int tid = threadIdx.x, lane = tid & 63, wid = tid >> 6;
  const int rt = blockIdx.x >> 4, ct = blockIdx.x & 15;
  const int row0 = rt * BM, col0 = ct * BN;
  float m_b[4], inv[4];
#pragma unroll
  for (int i = 0; i < 4; ++i) {
    int r = row0 + (tid >> 3) + 32 * i;
    ln_combine8<80>(st[r * 8 + (tid & 7)], m_b[i], inv[i]);
  }
  int boff[C::B_PER];
#pragma unroll
  for (int i = 0; i < C::B_PER; ++i) {
    int id = tid + 256 * i, kr = id / C::CPR, c = id % C::CPR;
    boff[i] = kr * F2_ + col0 + c * 8;
  }
  const u16* ap = x + (size_t)(row0 + (tid >> 3)) * D_ + (tid & 7) * 8;
  auto ald = [&](int it, int i) { return *(const uint4*)(ap + (size_t)i * 32 * D_ + it * 64); };
  auto axf = [&](uint4 r, int it, int i) { return ln_chunk(r, m_b[i], inv[i]); };
  auto bld = [&](int it, int i) { return *(const uint4*)(win + (size_t)it * 64 * F2_ + boff[i]); };
  auto bxf = [&](uint4 r, int it, int i) { return r; };
  f32x4 acc[4][3];
  mainloop<BM, BN, 2, 2, D_>(smem, ald, axf, bld, bxf, acc);
  const int g = lane >> 4, c16 = lane & 15;
  const int wm = wid >> 1, wn = wid & 1;
  const bool second = ct >= 8;
  u16* dst = second ? y2 : z1;
  const int cbase = (second ? col0 - FH_ : col0) + wn * 48 + c16;
  float* red = (float*)smem;
#pragma unroll
  for (int m = 0; m < 4; ++m)
#pragma unroll
    for (int i = 0; i < 4; ++i) {
      int r = wm * 64 + m * 16 + 4 * g + i;
      float zv[3];
#pragma unroll
      for (int n = 0; n < 3; ++n) {
        float y = rbf(acc[m][n][i]);
        float z = rbf(gelu_tanh(y));
        zv[n] = z;
        dst[(size_t)(row0 + r) * FH_ + cbase + 16 * n] = f2bf(z);
      }
      if (second) {
        float s = zv[0] + zv[1] + zv[2];
        s += __shfl_xor(s, 1); s += __shfl_xor(s, 2); s += __shfl_xor(s, 4); s += __shfl_xor(s, 8);
        float mu = s * (1.0f / 48.0f), m2 = 0.f;
#pragma unroll
        for (int n = 0; n < 3; ++n) m2 += (zv[n] - mu) * (zv[n] - mu);
        m2 += __shfl_xor(m2, 1); m2 += __shfl_xor(m2, 2); m2 += __shfl_xor(m2, 4); m2 += __shfl_xor(m2, 8);
        if (c16 == 0) { red[(r * 2 + wn) * 2] = mu; red[(r * 2 + wn) * 2 + 1] = m2; }
      }
    }
  if (second) {
    __syncthreads();
    if (tid < 128) {
      float m0 = red[tid * 4], q0 = red[tid * 4 + 1], m1 = red[tid * 4 + 2], q1 = red[tid * 4 + 3];
      float mu = 0.5f * (m0 + m1), dm = m0 - m1;
      yst[(row0 + tid) * 8 + (ct - 8)] = make_float2(mu, q0 + q1 + 24.0f * dm * dm);
    }
  }
}

__global__ __launch_bounds__(256) void k_spatial(const u16* __restrict__ wsp, const u16* __restrict__ bsp,
                                                 const u16* __restrict__ y2, const float2* __restrict__ yst,
                                                 const u16* __restrict__ z1, u16* __restrict__ u) {
  extern __shared__ __attribute__((aligned(16))) char smem[];
  constexpr int BM = 64, BN = 96;
  using C = GemmCfg<BM, BN>;
  const int tid = threadIdx.x, lane = tid & 63, wid = tid >> 6;
  const int rt = blockIdx.x >> 3, ct = blockIdx.x & 7;
  const int row0 = rt * BM, col0 = ct * BN;
  float2* lnp = (float2*)(smem + 2 * C::STAGE);
  for (int r8 = tid >> 3; r8 < S_; r8 += 32) {
    float m_b, inv;
    ln_combine8<96>(yst[r8 * 8 + (tid & 7)], m_b, inv);
    if ((tid & 7) == 0) lnp[r8] = make_float2(m_b, inv);
  }
  __syncthreads();
  int boff[C::B_PER], bkr[C::B_PER];
#pragma unroll
  for (int i = 0; i < C::B_PER; ++i) {
    int id = tid + 256 * i, kr = id / C::CPR, c = id % C::CPR;
    boff[i] = kr * FH_ + col0 + c * 8;
    bkr[i] = kr;
  }
  const u16* ap = wsp + (size_t)(row0 + (tid >> 3)) * S_ + (tid & 7) * 8;
  auto ald = [&](int it, int i) { return *(const uint4*)(ap + (size_t)i * 32 * S_ + it * 64); };
  auto axf = [&](uint4 r, int it, int i) { return r; };
  auto bld = [&](int it, int i) { return *(const uint4*)(y2 + (size_t)it * 64 * FH_ + boff[i]); };
  auto bxf = [&](uint4 r, int it, int i) {
    float2 pr = lnp[it * 64 + bkr[i]];
    return ln_chunk(r, pr.x, pr.y);
  };
  f32x4 acc[2][3];
  mainloop<BM, BN, 2, 2, S_>(smem, ald, axf, bld, bxf, acc);
  const int g = lane >> 4, c16 = lane & 15;
  const int wm = wid >> 1, wn = wid & 1;
#pragma unroll
  for (int m = 0; m < 2; ++m)
#pragma unroll
    for (int i = 0; i < 4; ++i) {
      int r = row0 + wm * 32 + m * 16 + 4 * g + i;
      float b = bf2f(bsp[r]);
#pragma unroll
      for (int n = 0; n < 3; ++n) {
        size_t idx = (size_t)r * FH_ + col0 + wn * 48 + 16 * n + c16;
        float gg = rbf(rbf(acc[m][n][i]) + b);
        u[idx] = f2bf(bf2f(z1[idx]) * gg);
      }
    }
}

extern "C" void kernel_launch(void* const* d_in, const int* in_sizes, int n_in, void* d_out, int out_size,
                              void* d_ws, size_t ws_size, hipStream_t stream) {
  const u16* A_x = (const u16*)d_in[0];
  const u16* A_wqkv = (const u16*)d_in[1];
  const u16* A_wphi = (const u16*)d_in[2];
  const u16* A_wo = (const u16*)d_in[3];
  const u16* B_win = (const u16*)d_in[5];
  const u16* B_wsp = (const u16*)d_in[6];
  const u16* B_bsp = (const u16*)d_in[7];
  const u16* B_wout = (const u16*)d_in[8];
  u16* out = (u16*)d_out;

  char* ws = (char*)d_ws;
  size_t off = 0;
  auto alloc = [&](size_t bytes) { char* p = ws + off; off += (bytes + 255) & ~(size_t)255; return p; };
  u16* xa = (u16*)alloc((size_t)S_ * D_ * 2);
  u16* xb = (u16*)alloc((size_t)S_ * D_ * 2);
  float2* st0 = (float2*)alloc((size_t)S_ * 8 * 8);
  float2* stA = (float2*)alloc((size_t)S_ * 8 * 8);
  float2* stB = (float2*)alloc((size_t)S_ * 8 * 8);
  float2* yst = (float2*)alloc((size_t)S_ * 8 * 8);
  u16* phiq = (u16*)alloc((size_t)S_ * 1280 * 2);
  float* kvpart = (float*)alloc((size_t)16 * H_ * 40 * KVP * 4);
  u16* o = (u16*)alloc((size_t)S_ * D_ * 2);
  u16* z1 = (u16*)alloc((size_t)S_ * FH_ * 2);
  u16* y2 = (u16*)alloc((size_t)S_ * FH_ * 2);
  u16* u = (u16*)alloc((size_t)S_ * FH_ * 2);

  constexpr int SM_QKV = (2 * GemmCfg<128, 160>::STAGE > 128 * (TSTR + PSTRIDE) * 2) ? 2 * GemmCfg<128, 160>::STAGE
                                                                                       : 128 * (TSTR + PSTRIDE) * 2;
  constexpr int SM_RES = 2 * GemmCfg<64, 80>::STAGE;
  constexpr int SM_WIN = 2 * GemmCfg<128, 96>::STAGE;
  constexpr int SM_SP = 2 * GemmCfg<64, 96>::STAGE + S_ * 8;
  hipFuncSetAttribute((const void*)k_qkv, hipFuncAttributeMaxDynamicSharedMemorySize, SM_QKV);
  hipFuncSetAttribute((const void*)k_res_gemm<D_>, hipFuncAttributeMaxDynamicSharedMemorySize, SM_RES);
  hipFuncSetAttribute((const void*)k_res_gemm<FH_>, hipFuncAttributeMaxDynamicSharedMemorySize, SM_RES);
  hipFuncSetAttribute((const void*)k_win, hipFuncAttributeMaxDynamicSharedMemorySize, SM_WIN);
  hipFuncSetAttribute((const void*)k_spatial, hipFuncAttributeMaxDynamicSharedMemorySize, SM_SP);

  k_stats0<<<S_ / 4, 256, 0, stream>>>(A_x, st0);
  const u16* xcur = A_x;
  const float2* stcur = st0;
  for (int layer = 0; layer < 2; ++layer) {
    k_qkv<<<16 * 12, 256, SM_QKV, stream>>>(xcur, stcur, A_wqkv, A_wphi, phiq, kvpart);
    k_o<<<32 * 8, 256, 0, stream>>>(kvpart, phiq, o);
    k_res_gemm<D_><<<32 * 8, 256, SM_RES, stream>>>(o, A_wo, xcur, xa, stA);
    k_win<<<16 * 16, 256, SM_WIN, stream>>>(xa, stA, B_win, z1, y2, yst);
    k_spatial<<<32 * 8, 256, SM_SP, stream>>>(B_wsp, B_bsp, y2, yst, z1, u);
    u16* xo = layer == 0 ? xb : out;
    k_res_gemm<FH_><<<32 * 8, 256, SM_RES, stream>>>(u, B_wout, xa, xo, layer == 0 ? stB : nullptr);
    xcur = xb;
    stcur = stB;
  }
}
```

```cpp
#include <hip/hip_runtime.h>
#include <stdint.h>

typedef unsigned short u16;
typedef __attribute__((ext_vector_type(8))) short s16x8;
typedef __attribute__((ext_vector_type(4))) short s16x4;
typedef __attribute__((ext_vector_type(4))) float f32x4;
typedef __attribute__((ext_vector_type(16))) float f32x16;
typedef __attribute__((address_space(3))) s16x4* lds_s16x4_ptr;

#define STAMP(k)

constexpr int S_ = 2048, D_ = 640, H_ = 32, DH_ = 20, D3_ = 1920, FH_ = 768, F2_ = 1536;

__device__ __forceinline__ float bf2f(u16 v) { return __uint_as_float(((unsigned)v) << 16); }
__device__ __forceinline__ u16 f2bf(float f) { return __builtin_bit_cast(u16, (__bf16)f); }
__device__ __forceinline__ float rbf(float f) { return bf2f(f2bf(f)); }
__device__ __forceinline__ unsigned pack2(float a, float b) { return (unsigned)f2bf(a) | ((unsigned)f2bf(b) << 16); }
__device__ __forceinline__ float lo_f(unsigned w) { return __uint_as_float(w << 16); }
__device__ __forceinline__ float hi_f(unsigned w) { return __uint_as_float(w & 0xffff0000u); }

__device__ __forceinline__ f32x4 mfma16(s16x8 a, s16x8 b, f32x4 c) {
  return __builtin_amdgcn_mfma_f32_16x16x32_bf16(a, b, c, 0, 0, 0);
}
__device__ __forceinline__ f32x16 mfma32(s16x8 a, s16x8 b, f32x16 c) {
  return __builtin_amdgcn_mfma_f32_32x32x16_bf16(a, b, c, 0, 0, 0);
}
__device__ __forceinline__ s16x4 tr_read(const char* p) {
  return __builtin_amdgcn_ds_read_tr16_b64_v4i16((lds_s16x4_ptr)(uintptr_t)(unsigned)(uintptr_t)p);
}
__device__ __forceinline__ s16x8 cat8(s16x4 a, s16x4 b) {
  s16x8 r;
  r[0] = a[0]; r[1] = a[1]; r[2] = a[2]; r[3] = a[3];
  r[4] = b[0]; r[5] = b[1]; r[6] = b[2]; r[7] = b[3];
  return r;
}

template <int RT, int CT, int R, int C>
__device__ __forceinline__ void tile_map(int b, int& rt, int& ct) {
  static_assert(R * C * 8 == RT * CT && (RT / R) * (CT / C) == 8, "bad xcd map");
  int x = b & 7, idx = b >> 3;
  rt = (x / (CT / C)) * R + idx / C;
  ct = (x % (CT / C)) * C + idx % C;
}

__device__ __forceinline__ void ln_finish(float mean, float var, float& m_b, float& inv) {
  m_b = rbf(mean);
  float v_b = rbf(var);
  float den = rbf(sqrtf(rbf(v_b + 1e-5f)));
  inv = 1.0f / den;
}
template <int CNT>
__device__ __forceinline__ void ln_combine8(float2 part, float& m_b, float& inv) {
  float s = part.x;
  s += __shfl_xor(s, 1); s += __shfl_xor(s, 2); s += __shfl_xor(s, 4);
  float mu = s * 0.125f;
  float dm = part.x - mu;
  float m2 = part.y + (float)CNT * dm * dm;
  m2 += __shfl_xor(m2, 1); m2 += __shfl_xor(m2, 2); m2 += __shfl_xor(m2, 4);
  ln_finish(mu, m2 * (1.0f / (8 * CNT)), m_b, inv);
}

__device__ __forceinline__ uint4 ln_chunk(uint4 raw, float sc, float sh) {
  uint4 r;
  unsigned* s = (unsigned*)&raw;
  unsigned* d = (unsigned*)&r;
#pragma unroll
  for (int i = 0; i < 4; ++i) d[i] = pack2(fmaf(lo_f(s[i]), sc, sh), fmaf(hi_f(s[i]), sc, sh));
  return r;
}

template <int BM, int BN, int BK, int NTH>
struct GemmCfg {
  static constexpr int PSTR = BK * 64;
  static constexpr int NW = NTH / 64;
  static constexpr int NP0 = (BN + 31) / 32;
  static constexpr int NPANEL = ((NP0 * PSTR / 1024) % NW == 0) ? NP0 : NP0 + 1;
  static constexpr int A_BYTES = BM * BK * 2;
  static constexpr int B_BYTES = NPANEL * PSTR;
  static constexpr int STAGE = A_BYTES + B_BYTES;
  static constexpr int A_INS = A_BYTES / 1024 / NW;
  static constexpr int B_INS = B_BYTES / 1024 / NW;
  static constexpr int NS = 3;
  static_assert(A_INS * 1024 * NW == A_BYTES && B_INS * 1024 * NW == B_BYTES, "DMA tiling must divide evenly");
};

__device__ __forceinline__ void dma16(const void* g, char* l) {
  __builtin_amdgcn_global_load_lds((const unsigned*)g, (__attribute__((address_space(3))) unsigned*)(uintptr_t)(unsigned)(uintptr_t)l, 16, 0, 0);
}

template <int BM, int BN, int BK, int NTH, int WGM, int WGN, int KTOT, class BCol>
__device__ __forceinline__ void mainloop(char* smem, const u16* __restrict__ A, int lda, const u16* __restrict__ B, int ldb,
                                         BCol bcol, f32x4 (&acc)[BM / WGM / 16][BN / WGN / 16]) {
  using C = GemmCfg<BM, BN, BK, NTH>;
  constexpr int MT = BM / WGM / 16, NT = BN / WGN / 16, NIT = KTOT / BK;
  constexpr int CA = BK / 8;
  const int tid = threadIdx.x, lane = tid & 63, wid = tid >> 6;
  const int wm = wid / WGN, wn = wid % WGN;
  const int g = lane >> 4, c16 = lane & 15;

  int aoff[C::A_INS], boff[C::B_INS];
#pragma unroll
  for (int i = 0; i < C::A_INS; ++i) {
    int pos = (wid * C::A_INS + i) * 64 + lane;
    int row = pos / CA, cp = pos % CA;
    aoff[i] = row * lda + ((cp ^ (row & 7)) << 3);
  }
#pragma unroll
  for (int i = 0; i < C::B_INS; ++i) {
    int pos = (wid * C::B_INS + i) * 64 + lane;
    int panel = pos / (BK * 4), rem = pos % (BK * 4), kr = rem >> 2, cp = rem & 3;
    int c = panel * 4 + (cp ^ (((kr >> 3) & 1) << 1));
    boff[i] = kr * ldb + bcol(c);
  }
  auto issue = [&](int it) {
    char* buf = smem + (it % C::NS) * C::STAGE;
#pragma unroll
    for (int i = 0; i < C::A_INS; ++i) dma16(A + aoff[i] + it * BK, buf + (wid * C::A_INS + i) * 1024);
#pragma unroll
    for (int i = 0; i < C::B_INS; ++i)
      dma16(B + boff[i] + (size_t)it * BK * ldb, buf + C::A_BYTES + (wid * C::B_INS + i) * 1024);
  };

#pragma unroll
  for (int m = 0; m < MT; ++m)
#pragma unroll
    for (int n = 0; n < NT; ++n) acc[m][n] = f32x4{0.f, 0.f, 0.f, 0.f};

  issue(0);
  issue(1 < NIT ? 1 : 0);
  const int a_off = (wm * MT * 16 + c16) * (BK * 2);
  const int q = c16 >> 2, p4 = lane & 3;
  const int b_off = C::A_BYTES + (8 * g + q) * 64 + p4 * 8;

  for (int it = 0; it < NIT; ++it) {
    if constexpr (C::A_INS + C::B_INS == 4) asm volatile("s_waitcnt vmcnt(4)" ::: "memory");
    else if constexpr (C::A_INS + C::B_INS == 5) asm volatile("s_waitcnt vmcnt(5)" ::: "memory");
    else if constexpr (C::A_INS + C::B_INS == 9) asm volatile("s_waitcnt vmcnt(9)" ::: "memory");
    else if constexpr (C::A_INS + C::B_INS == 7) asm volatile("s_waitcnt vmcnt(7)" ::: "memory");
    else if constexpr (C::A_INS + C::B_INS == 10) asm volatile("s_waitcnt vmcnt(10)" ::: "memory");
    else static_assert(C::A_INS + C::B_INS == -1, "add a vmcnt case");
    __builtin_amdgcn_s_barrier();
    issue(it + 2 < NIT ? it + 2 : NIT - 1);
    const char* buf = smem + (it % C::NS) * C::STAGE;
#pragma unroll
    for (int ks = 0; ks < BK / 32; ++ks) {
      s16x8 af[MT], bfr[NT];
#pragma unroll
      for (int m = 0; m < MT; ++m)
        af[m] = *(const s16x8*)(buf + a_off + m * 16 * (BK * 2) + (((4 * ks + g) ^ (c16 & 7)) << 4));
#pragma unroll
      for (int n = 0; n < NT; ++n) {
        int nb = wn * NT + n;
        const char* p = buf + b_off + (nb >> 1) * C::PSTR + ks * 32 * 64 + (((nb & 1) ^ (g & 1)) << 5);
        bfr[n] = cat8(tr_read(p), tr_read(p + 4 * 64));
      }
#pragma unroll
      for (int m = 0; m < MT; ++m)
#pragma unroll
        for (int n = 0; n < NT; ++n) acc[m][n] = mfma16(bfr[n], af[m], acc[m][n]);
    }
  }
  asm volatile("s_waitcnt vmcnt(0)" ::: "memory");
  __syncthreads();
}

template <int MT, int NT, int STR>
__device__ __forceinline__ void acc_to_lds(u16* T, const f32x4 (&acc)[MT][NT], int row0, int col0) {
  const int lane = threadIdx.x & 63, g = lane >> 4, c16 = lane & 15;
#pragma unroll
  for (int m = 0; m < MT; ++m)
#pragma unroll
    for (int n = 0; n < NT; ++n)
      *(uint2*)(T + (row0 + m * 16 + c16) * STR + col0 + n * 16 + 4 * g) =
          make_uint2(pack2(acc[m][n][0], acc[m][n][1]), pack2(acc[m][n][2], acc[m][n][3]));
}

__device__ __forceinline__ void chunk_stats(const float (&y)[8], float& mu, float& m2) {
  float s = ((y[0] + y[1]) + (y[2] + y[3])) + ((y[4] + y[5]) + (y[6] + y[7]));
  mu = s * 0.125f;
  m2 = 0.f;
#pragma unroll
  for (int e = 0; e < 8; ++e) m2 += (y[e] - mu) * (y[e] - mu);
}

__global__ __launch_bounds__(256) void k_stats0(const u16* __restrict__ x, float2* __restrict__ st,
                                                const u16* __restrict__ wqkv, const u16* __restrict__ win,
                                                float* __restrict__ csq, float* __restrict__ csw) {
  if (blockIdx.x >= S_ / 4) {
    __shared__ float red[4][64];
    int cb = blockIdx.x - S_ / 4;
    const u16* w = cb < 30 ? wqkv : win;
    int n = cb < 30 ? D3_ : F2_;
    int col = (cb < 30 ? cb : cb - 30) * 64 + (threadIdx.x & 63);
    int rg = threadIdx.x >> 6;
    float acc = 0.f;
#pragma unroll 16
    for (int r = rg * 160; r < rg * 160 + 160; ++r) acc += bf2f(w[(size_t)r * n + col]);
    red[rg][threadIdx.x & 63] = acc;
    __syncthreads();
    if (threadIdx.x < 64) (cb < 30 ? csq : csw)[col] = (red[0][threadIdx.x] + red[1][threadIdx.x]) + (red[2][threadIdx.x] + red[3][threadIdx.x]);
    return;
  }
  int row = blockIdx.x * 4 + (threadIdx.x >> 6), lane = threadIdx.x & 63;
  const unsigned* p = (const unsigned*)(x + (size_t)row * D_ + lane * 10);
  float v[10];
#pragma unroll
  for (int i = 0; i < 5; ++i) {
    unsigned w = p[i];
    v[2 * i] = lo_f(w);
    v[2 * i + 1] = hi_f(w);
  }
  float s = 0.f;
#pragma unroll
  for (int i = 0; i < 10; ++i) s += v[i];
  s += __shfl_xor(s, 1); s += __shfl_xor(s, 2); s += __shfl_xor(s, 4);
  float mu = s * (1.0f / 80.0f), m2 = 0.f;
#pragma unroll
  for (int i = 0; i < 10; ++i) m2 += (v[i] - mu) * (v[i] - mu);
  m2 += __shfl_xor(m2, 1); m2 += __shfl_xor(m2, 2); m2 += __shfl_xor(m2, 4);
  if ((lane & 7) == 0) st[row * 8 + (lane >> 3)] = make_float2(mu, m2);
}

__device__ __forceinline__ float2 row_ln(const float2* __restrict__ st, int row) {
  const float4* p = (const float4*)(st + (size_t)row * 8);
  float4 a = p[0], b = p[1], c = p[2], d = p[3];
  float mu = ((a.x + a.z) + (b.x + b.z) + (c.x + c.z) + (d.x + d.z)) * 0.125f;
  float m2 = (a.y + a.w) + (b.y + b.w) + (c.y + c.w) + (d.y + d.w);
  float e;
  e = a.x - mu; m2 += 80.f * e * e; e = a.z - mu; m2 += 80.f * e * e;
  e = b.x - mu; m2 += 80.f * e * e; e = b.z - mu; m2 += 80.f * e * e;
  e = c.x - mu; m2 += 80.f * e * e; e = c.z - mu; m2 += 80.f * e * e;
  e = d.x - mu; m2 += 80.f * e * e; e = d.z - mu; m2 += 80.f * e * e;
  float m_b, inv;
  ln_finish(mu, m2 * (1.0f / 640.0f), m_b, inv);
  return make_float2(inv, m_b * inv);
}

constexpr int TSTR = 176;
constexpr int PSTRIDE = 168;
constexpr int KVP = 24;
__global__ __launch_bounds__(512) void k_qkv(const u16* __restrict__ x, const float2* __restrict__ st,
                                             const u16* __restrict__ wqkv, const u16* __restrict__ wphi,
                                             u16* __restrict__ phiq, float* __restrict__ kvpart, const float* __restrict__ csq) {
  extern __shared__ __attribute__((aligned(16))) char smem[];
  constexpr int BM = 128, BN = 160, BK = 64, NTH = 512;
  using C = GemmCfg<BM, BN, BK, NTH>;
  const int tid = threadIdx.x, lane = tid & 63, wid = tid >> 6;
  int rt, ct;
  tile_map<16, 12, 4, 6>(blockIdx.x, rt, ct);
  const bool isq = ct < 4;
  const int row0 = rt * BM;

  auto bcol = [&](int c) { return c >= 20 ? 0 : (isq ? ct * 160 + c * 8 : (c < 10 ? D_ + (ct - 4) * 80 + c * 8 : 2 * D_ + (ct - 4) * 80 + (c - 10) * 8)); };
  float2 lnr[2];
#pragma unroll
  for (int m = 0; m < 2; ++m) lnr[m] = row_ln(st, row0 + (wid >> 1) * 32 + m * 16 + (lane & 15));
  const int hh = lane >> 5, c32 = lane & 31;
  const int j = isq ? wid : (wid & 3);
  const int h = isq ? ct * 8 + j : (ct - 4) * 4 + j;

  f32x4 acc[2][5];
  mainloop<BM, BN, BK, NTH, 4, 2, D_>(smem, x + (size_t)row0 * D_, D_, wqkv, D3_, bcol, acc);
  {
#pragma unroll
    for (int n = 0; n < 5; ++n) {
      int cc = (wid & 1) * 10 + n * 2 + ((lane >> 4) >> 1);
      f32x4 cs = *(const f32x4*)(csq + bcol(cc) + 4 * ((lane >> 4) & 1));
#pragma unroll
      for (int m = 0; m < 2; ++m)
#pragma unroll
        for (int i = 0; i < 4; ++i) acc[m][n][i] = fmaf(acc[m][n][i], lnr[m].x, -lnr[m].y * cs[i]);
    }
  }
  s16x8 wA[2];
#pragma unroll
  for (int ks = 0; ks < 2; ++ks) {
#pragma unroll
    for (int e = 0; e < 8; ++e) {
      int d = 16 * ks + 8 * hh + e;
      wA[ks][e] = (c32 < DH_ && d < DH_) ? (short)wphi[(h * DH_ + d) * DH_ + c32] : (short)0;
    }
  }

  u16* T = (u16*)smem;
  u16* P = (u16*)(smem + 128 * TSTR * 2);
  acc_to_lds<2, 5, TSTR>(T, acc, (wid >> 1) * 32, (wid & 1) * 80);
  __syncthreads();
  {
    const int nmt = isq ? 4 : 2;
#pragma unroll 2
    for (int mi = 0; mi < 4; ++mi) {
      if (mi < nmt) {
        const int mt = isq ? mi : 2 * (wid >> 2) + mi;
        const u16* src = T + (mt * 32 + c32) * TSTR + 20 * j + 8 * hh;
        s16x8 b0 = cat8(*(const s16x4*)src, *(const s16x4*)(src + 4));
        s16x8 b1 = cat8(*(const s16x4*)(src + 16), *(const s16x4*)(src + 20));
        f32x16 pp;
#pragma unroll
        for (int r = 0; r < 16; ++r) pp[r] = 0.f;
        pp = mfma32(wA[0], b0, pp);
        pp = mfma32(wA[1], b1, pp);
        float a[12];
#pragma unroll
        for (int r = 0; r < 12; ++r) a[r] = rbf(pp[r]);
        float mx = a[0], mn = a[0];
#pragma unroll
        for (int r = 1; r < 8; ++r) { mx = fmaxf(mx, a[r]); mn = fminf(mn, a[r]); }
        if (hh == 0) {
#pragma unroll
          for (int r = 8; r < 12; ++r) { mx = fmaxf(mx, a[r]); mn = fminf(mn, a[r]); }
        }
        mx = fmaxf(mx, __shfl_xor(mx, 32));
        mn = fminf(mn, __shfl_xor(mn, 32));
        float e1[12], e2[12], s1 = 0.f, s2 = 0.f;
#pragma unroll
        for (int r = 0; r < 12; ++r) {
          e1[r] = __expf(a[r] - mx);
          e2[r] = __expf(mn - a[r]);
          if (r < 8 || hh == 0) { s1 += e1[r]; s2 += e2[r]; }
        }
        s1 += __shfl_xor(s1, 32);
        s2 += __shfl_xor(s2, 32);
        float r1 = 1.0f / s1, r2 = 1.0f / s2;
        u16* dst = isq ? phiq + (size_t)(row0 + mt * 32 + c32) * 1280 + h * 40 : P + (mt * 32 + c32) * PSTRIDE + 40 * j;
#pragma unroll
        for (int qd = 0; qd < 3; ++qd) {
          if (qd < 2 || hh == 0) {
            int f0 = 8 * qd + 4 * hh;
            *(uint2*)(dst + f0) = make_uint2(pack2(e1[4 * qd] * r1, e1[4 * qd + 1] * r1),
                                             pack2(e1[4 * qd + 2] * r1, e1[4 * qd + 3] * r1));
            *(uint2*)(dst + 20 + f0) = make_uint2(pack2(e2[4 * qd] * r2, e2[4 * qd + 1] * r2),
                                                  pack2(e2[4 * qd + 2] * r2, e2[4 * qd + 3] * r2));
          }
        }
      }
    }
  }
  if (isq) return;
  __syncthreads();
  if (wid >= 4) return;
  {
    const int g = lane >> 4, c16 = lane & 15;
    const int q = c16 >> 2, p4 = lane & 3;
    f32x4 kacc[3][2];
#pragma unroll
    for (int a = 0; a < 3; ++a)
#pragma unroll
      for (int b = 0; b < 2; ++b) kacc[a][b] = f32x4{0.f, 0.f, 0.f, 0.f};
    s16x8 ones;
#pragma unroll
    for (int e = 0; e < 8; ++e) ones[e] = (c16 == 4) ? (short)0x3F80 : (short)0;
#pragma unroll
    for (int ks = 0; ks < 4; ++ks) {
      int r0 = 32 * ks + 8 * g + q;
      s16x8 af[3], bfr[2];
#pragma unroll
      for (int ft = 0; ft < 3; ++ft) {
        const char* p = (const char*)(P + r0 * PSTRIDE + 40 * j + 16 * ft + 4 * p4);
        af[ft] = cat8(tr_read(p), tr_read(p + 4 * PSTRIDE * 2));
      }
#pragma unroll
      for (int nt = 0; nt < 2; ++nt) {
        const char* p = (const char*)(T + r0 * TSTR + 80 + 20 * j + 16 * nt + 4 * p4);
        bfr[nt] = cat8(tr_read(p), tr_read(p + 4 * TSTR * 2));
      }
      if (c16 >= 4) bfr[1] = ones;
#pragma unroll
      for (int ft = 0; ft < 3; ++ft)
#pragma unroll
        for (int nt = 0; nt < 2; ++nt) kacc[ft][nt] = mfma16(af[ft], bfr[nt], kacc[ft][nt]);
    }
    float* dst = kvpart + (size_t)(rt * H_ + h) * 40 * KVP;
#pragma unroll
    for (int ft = 0; ft < 3; ++ft)
#pragma unroll
      for (int nt = 0; nt < 2; ++nt)
#pragma unroll
        for (int i = 0; i < 4; ++i) {
          int f = 16 * ft + 4 * g + i, d = 16 * nt + c16;
          if (f < 40 && d <= 20) dst[f * KVP + d] = kacc[ft][nt][i];
        }
  }
}

__global__ __launch_bounds__(256) void k_o(const float* __restrict__ kvpart, const u16* __restrict__ phiq,
                                           u16* __restrict__ o) {
  __shared__ __attribute__((aligned(16))) u16 KV[32 * 64];
  const int tid = threadIdx.x, lane = tid & 63, wid = tid >> 6;
  const int h = blockIdx.x >> 3, tt = blockIdx.x & 7;
  const int g = lane >> 4, c16 = lane & 15;
  s16x8 b0[4], b1[4];
#pragma unroll
  for (int nt = 0; nt < 4; ++nt) {
    int tok = tt * 256 + wid * 64 + nt * 16 + c16;
    const u16* src = phiq + (size_t)tok * 1280 + h * 40 + 8 * g;
    b0[nt] = *(const s16x8*)src;
    if (g == 0) b1[nt] = *(const s16x8*)(src + 32);
    else {
#pragma unroll
      for (int e = 0; e < 8; ++e) b1[nt][e] = 0;
    }
  }
  float s[4];
#pragma unroll
  for (int i = 0; i < 4; ++i) {
    int e = tid + 256 * i;
    s[i] = 0.f;
    if (e < 40 * KVP) {
      const float* p = kvpart + (size_t)h * 40 * KVP + e;
#pragma unroll
      for (int r = 0; r < 16; ++r) s[i] += p[(size_t)r * H_ * 40 * KVP];
    }
  }
  for (int e = tid; e < 32 * 64; e += 256) KV[e] = 0;
  __syncthreads();
#pragma unroll
  for (int i = 0; i < 4; ++i) {
    int e = tid + 256 * i;
    int f = e / KVP, d = e % KVP;
    if (e < 40 * KVP && d <= 20) KV[d * 64 + f] = f2bf(s[i]);
  }
  __syncthreads();
  s16x8 af[2][2];
#pragma unroll
  for (int mt = 0; mt < 2; ++mt)
#pragma unroll
    for (int ks = 0; ks < 2; ++ks) af[mt][ks] = *(const s16x8*)(KV + (16 * mt + c16) * 64 + 32 * ks + 8 * g);
#pragma unroll
  for (int nt = 0; nt < 4; ++nt) {
    int tok = tt * 256 + wid * 64 + nt * 16 + c16;
    f32x4 z4 = f32x4{0.f, 0.f, 0.f, 0.f};
    f32x4 c0 = mfma16(af[0][0], b0[nt], z4);
    c0 = mfma16(af[0][1], b1[nt], c0);
    f32x4 c1 = mfma16(af[1][0], b0[nt], z4);
    c1 = mfma16(af[1][1], b1[nt], c1);
    float den = __shfl(c1[0], 16 + c16);
    den = rbf(rbf(den) + 1e-6f);
    u16* dst = o + (size_t)tok * D_ + h * DH_;
    *(uint2*)(dst + 4 * g) = make_uint2(pack2(rbf(c0[0]) / den, rbf(c0[1]) / den), pack2(rbf(c0[2]) / den, rbf(c0[3]) / den));
    if (g == 0)
      *(uint2*)(dst + 16) = make_uint2(pack2(rbf(c1[0]) / den, rbf(c1[1]) / den), pack2(rbf(c1[2]) / den, rbf(c1[3]) / den));
  }
}

template <int KTOT>
__global__ __launch_bounds__(256) void k_res_gemm(const u16* __restrict__ a, const u16* __restrict__ w,
                                                  const u16* __restrict__ xin, u16* __restrict__ xo,
                                                  float2* __restrict__ st) {
  extern __shared__ __attribute__((aligned(16))) char smem[];
  constexpr int BM = 64, BN = 80, BK = 128, NTH = 256, STR = 88;
  using C = GemmCfg<BM, BN, BK, NTH>;
  const int tid = threadIdx.x, lane = tid & 63, wid = tid >> 6;
  int rt, ct;
  tile_map<32, 8, 8, 4>(blockIdx.x, rt, ct);
  const int row0 = rt * BM, col0 = ct * BN;
  uint4 xr[3];
#pragma unroll
  for (int i = 0; i < 3; ++i) {
    int id = tid + 256 * i;
    if (id < 640) xr[i] = *(const uint4*)(xin + (size_t)(row0 + id / 10) * D_ + col0 + (id % 10) * 8);
  }
  auto bcol = [&](int c) { return c < 10 ? col0 + c * 8 : col0; };
  f32x4 acc[1][5];
  mainloop<BM, BN, BK, NTH, 4, 1, KTOT>(smem, a + (size_t)row0 * KTOT, KTOT, w, D_, bcol, acc);
  u16* T = (u16*)smem;
  float2* cs = (float2*)(smem + BM * STR * 2);
  acc_to_lds<1, 5, STR>(T, acc, wid * 16, 0);
  __syncthreads();
#pragma unroll
  for (int i = 0; i < 3; ++i) {
    int id = tid + 256 * i;
    if (id < 640) {
      int r = id / 10, c = id % 10;
      uint4 tv = *(const uint4*)(T + r * STR + c * 8);
      const unsigned* tp = (const unsigned*)&tv;
      const unsigned* xp = (const unsigned*)&xr[i];
      float y[8];
#pragma unroll
      for (int e = 0; e < 4; ++e) {
        y[2 * e] = rbf(lo_f(xp[e]) + lo_f(tp[e]));
        y[2 * e + 1] = rbf(hi_f(xp[e]) + hi_f(tp[e]));
      }
      uint4 ov = make_uint4(pack2(y[0], y[1]), pack2(y[2], y[3]), pack2(y[4], y[5]), pack2(y[6], y[7]));
      *(uint4*)(xo + (size_t)(row0 + r) * D_ + col0 + c * 8) = ov;
      float mu, m2;
      chunk_stats(y, mu, m2);
      cs[r * 10 + c] = make_float2(mu, m2);
    }
  }
  if (st != nullptr) {
    __syncthreads();
    if (tid < BM) {
      float mu = 0.f;
#pragma unroll
      for (int c = 0; c < 10; ++c) mu += cs[tid * 10 + c].x;
      mu *= 0.1f;
      float m2 = 0.f;
#pragma unroll
      for (int c = 0; c < 10; ++c) {
        float2 v = cs[tid * 10 + c];
        m2 += v.y + 8.0f * (v.x - mu) * (v.x - mu);
      }
      st[(row0 + tid) * 8 + ct] = make_float2(mu, m2);
    }
  }
}

__device__ __forceinline__ float gelu_tanh(float y) {
  float u = 0.7978845608028654f * (y + 0.044715f * y * y * y);
  float e = __expf(2.0f * u);
  float th = 1.0f - 2.0f / (e + 1.0f);
  return 0.5f * y * (1.0f + th);
}
__global__ __launch_bounds__(512) void k_win(const u16* __restrict__ x, const float2* __restrict__ st,
                                             const u16* __restrict__ win, u16* __restrict__ z1,
                                             u16* __restrict__ y2, float2* __restrict__ yst, const float* __restrict__ csw) {
  extern __shared__ __attribute__((aligned(16))) char smem[];
  constexpr int BM = 128, BN = 96, BK = 64, NTH = 512, STR = 104;
  using C = GemmCfg<BM, BN, BK, NTH>;
  const int tid = threadIdx.x, lane = tid & 63, wid = tid >> 6;
  int rt, ct;
  tile_map<16, 16, 4, 8>(blockIdx.x, rt, ct);
  const int row0 = rt * BM, col0 = ct * BN;
  float2 lnr[2];
#pragma unroll
  for (int m = 0; m < 2; ++m) lnr[m] = row_ln(st, row0 + (wid >> 1) * 32 + m * 16 + (lane & 15));
  auto bcol = [&](int c) { return c < 12 ? col0 + c * 8 : col0; };
  f32x4 acc[2][3];
  mainloop<BM, BN, BK, NTH, 4, 2, D_>(smem, x + (size_t)row0 * D_, D_, win, F2_, bcol, acc);
#pragma unroll
  for (int n = 0; n < 3; ++n) {
    f32x4 cs = *(const f32x4*)(csw + col0 + (wid & 1) * 48 + n * 16 + 4 * (lane >> 4));
#pragma unroll
    for (int m = 0; m < 2; ++m)
#pragma unroll
      for (int i = 0; i < 4; ++i) acc[m][n][i] = fmaf(acc[m][n][i], lnr[m].x, -lnr[m].y * cs[i]);
  }
  const bool second = ct >= 8;
  u16* dst = second ? y2 : z1;
  const int cbase = second ? col0 - FH_ : col0;
#pragma unroll
  for (int m = 0; m < 2; ++m)
#pragma unroll
    for (int n = 0; n < 3; ++n)
#pragma unroll
      for (int i = 0; i < 4; ++i) acc[m][n][i] = gelu_tanh(rbf(acc[m][n][i]));
  u16* T = (u16*)smem;
  float2* cs = (float2*)(smem + BM * STR * 2);
  acc_to_lds<2, 3, STR>(T, acc, (wid >> 1) * 32, (wid & 1) * 48);
  __syncthreads();
#pragma unroll
  for (int i = 0; i < 3; ++i) {
    int id = tid + NTH * i, r = id / 12, c = id % 12;
    uint4 tv = *(const uint4*)(T + r * STR + c * 8);
    *(uint4*)(dst + (size_t)(row0 + r) * FH_ + cbase + c * 8) = tv;
    if (second) {
      const unsigned* tp = (const unsigned*)&tv;
      float y[8];
#pragma unroll
      for (int e = 0; e < 4; ++e) { y[2 * e] = lo_f(tp[e]); y[2 * e + 1] = hi_f(tp[e]); }
      float mu, m2;
      chunk_stats(y, mu, m2);
      cs[id] = make_float2(mu, m2);
    }
  }
  if (second) {
    __syncthreads();
    if (tid < BM) {
      float mu = 0.f;
#pragma unroll
      for (int c = 0; c < 12; ++c) mu += cs[tid * 12 + c].x;
      mu *= (1.0f / 12.0f);
      float m2 = 0.f;
#pragma unroll
      for (int c = 0; c < 12; ++c) {
        float2 v = cs[tid * 12 + c];
        m2 += v.y + 8.0f * (v.x - mu) * (v.x - mu);
      }
      yst[(row0 + tid) * 8 + (ct - 8)] = make_float2(mu, m2);
    }
  }
}

__global__ __launch_bounds__(256) void k_norm(u16* __restrict__ y2, const float2* __restrict__ yst) {
  int row = blockIdx.x * 4 + (threadIdx.x >> 6), lane = threadIdx.x & 63;
  u16* p = y2 + (size_t)row * FH_ + lane * 12;
  uint2 a = *(const uint2*)p, b = *(const uint2*)(p + 4), c = *(const uint2*)(p + 8);
  float m_b, inv;
  ln_combine8<96>(yst[row * 8 + (lane & 7)], m_b, inv);
  auto f = [&](unsigned w) { return pack2(rbf(lo_f(w) - m_b) * inv, rbf(hi_f(w) - m_b) * inv); };
  *(uint2*)p = make_uint2(f(a.x), f(a.y));
  *(uint2*)(p + 4) = make_uint2(f(b.x), f(b.y));
  *(uint2*)(p + 8) = make_uint2(f(c.x), f(c.y));
}

__global__ __launch_bounds__(512) void k_spatial(const u16* __restrict__ wsp, const u16* __restrict__ bsp,
                                                 const u16* __restrict__ y2,
                                                 const u16* __restrict__ z1, u16* __restrict__ u) {
  extern __shared__ __attribute__((aligned(16))) char smem[];
  constexpr int BM = 64, BN = 96, BK = 128, NTH = 512, STR = 104;
  using C = GemmCfg<BM, BN, BK, NTH>;
  const int tid = threadIdx.x, lane = tid & 63, wid = tid >> 6;
  int rt, ct;
#define DBG_SLOT 0
  STAMP(0);
  tile_map<32, 8, 8, 4>(blockIdx.x, rt, ct);
  const int row0 = rt * BM, col0 = ct * BN;
  auto bcol = [&](int c) { return col0 + c * 8; };
  f32x4 acc[1][3];
  STAMP(1);
  mainloop<BM, BN, BK, NTH, 4, 2, S_>(smem, wsp + (size_t)row0 * S_, S_, y2, FH_, bcol, acc);
  STAMP(2);
  u16* T = (u16*)smem;
  acc_to_lds<1, 3, STR>(T, acc, (wid >> 1) * 16, (wid & 1) * 48);
  __syncthreads();
#pragma unroll
  for (int i = 0; i < 2; ++i) {
    int id = tid + NTH * i, r = id / 12, c = id % 12;
    if (id < BM * 12) {
      uint4 tv = *(const uint4*)(T + r * STR + c * 8);
      size_t idx = (size_t)(row0 + r) * FH_ + col0 + c * 8;
      uint4 zv = *(const uint4*)(z1 + idx);
      float b = bf2f(bsp[row0 + r]);
      const unsigned* tp = (const unsigned*)&tv;
      const unsigned* zp = (const unsigned*)&zv;
      unsigned ov[4];
#pragma unroll
      for (int e = 0; e < 4; ++e)
        ov[e] = pack2(lo_f(zp[e]) * rbf(lo_f(tp[e]) + b), hi_f(zp[e]) * rbf(hi_f(tp[e]) + b));
      *(uint4*)(u + idx) = make_uint4(ov[0], ov[1], ov[2], ov[3]);
    }
  }
  STAMP(3);
#undef DBG_SLOT
}

extern "C" void kernel_launch(void* const* d_in, const int* in_sizes, int n_in, void* d_out, int out_size,
                              void* d_ws, size_t ws_size, hipStream_t stream) {
  const u16* A_x = (const u16*)d_in[0];
  const u16* A_wqkv = (const u16*)d_in[1];
  const u16* A_wphi = (const u16*)d_in[2];
  const u16* A_wo = (const u16*)d_in[3];
  const u16* B_win = (const u16*)d_in[5];
  const u16* B_wsp = (const u16*)d_in[6];
  const u16* B_bsp = (const u16*)d_in[7];
  const u16* B_wout = (const u16*)d_in[8];
  u16* out = (u16*)d_out;

  char* ws = (char*)d_ws;
  size_t off = 0;
  auto alloc = [&](size_t bytes) { char* p = ws + off; off += (bytes + 255) & ~(size_t)255; return p; };
  u16* xa = (u16*)alloc((size_t)S_ * D_ * 2);
  u16* xb = (u16*)alloc((size_t)S_ * D_ * 2);
  float2* st0 = (float2*)alloc((size_t)S_ * 8 * 8);
  float2* stA = (float2*)alloc((size_t)S_ * 8 * 8);
  float2* stB = (float2*)alloc((size_t)S_ * 8 * 8);
  float2* yst = (float2*)alloc((size_t)S_ * 8 * 8);
  u16* phiq = (u16*)alloc((size_t)S_ * 1280 * 2);
  float* kvpart = (float*)alloc((size_t)16 * H_ * 40 * KVP * 4);
  u16* o = (u16*)alloc((size_t)S_ * D_ * 2);
  u16* z1 = (u16*)alloc((size_t)S_ * FH_ * 2);
  u16* y2 = (u16*)alloc((size_t)S_ * FH_ * 2);
  u16* u = (u16*)alloc((size_t)S_ * FH_ * 2);
  float* csq = (float*)alloc(D3_ * 4);
  float* csw = (float*)alloc(F2_ * 4);

  constexpr int SM_QKV = (3 * GemmCfg<128, 160, 64, 512>::STAGE > 128 * (TSTR + PSTRIDE) * 2)
                             ? 3 * GemmCfg<128, 160, 64, 512>::STAGE
                             : 128 * (TSTR + PSTRIDE) * 2;
  constexpr int SM_RES = 3 * GemmCfg<64, 80, 128, 256>::STAGE;
  constexpr int SM_WIN = 3 * GemmCfg<128, 96, 64, 512>::STAGE;
  constexpr int SM_SP = 3 * GemmCfg<64, 96, 128, 512>::STAGE;
  hipFuncSetAttribute((const void*)k_qkv, hipFuncAttributeMaxDynamicSharedMemorySize, SM_QKV);
  hipFuncSetAttribute((const void*)k_res_gemm<D_>, hipFuncAttributeMaxDynamicSharedMemorySize, SM_RES);
  hipFuncSetAttribute((const void*)k_res_gemm<FH_>, hipFuncAttributeMaxDynamicSharedMemorySize, SM_RES);
  hipFuncSetAttribute((const void*)k_win, hipFuncAttributeMaxDynamicSharedMemorySize, SM_WIN);
  hipFuncSetAttribute((const void*)k_spatial, hipFuncAttributeMaxDynamicSharedMemorySize, SM_SP);

  k_stats0<<<S_ / 4 + 30 + 24, 256, 0, stream>>>(A_x, st0, A_wqkv, B_win, csq, csw);
  const u16* xcur = A_x;
  const float2* stcur = st0;
  for (int layer = 0; layer < 2; ++layer) {
    k_qkv<<<16 * 12, 512, SM_QKV, stream>>>(xcur, stcur, A_wqkv, A_wphi, phiq, kvpart, csq);
    k_o<<<32 * 8, 256, 0, stream>>>(kvpart, phiq, o);
    k_res_gemm<D_><<<32 * 8, 256, SM_RES, stream>>>(o, A_wo, xcur, xa, stA);
    k_win<<<16 * 16, 512, SM_WIN, stream>>>(xa, stA, B_win, z1, y2, yst, csw);
    k_norm<<<S_ / 4, 256, 0, stream>>>(y2, yst);
    k_spatial<<<32 * 8, 512, SM_SP, stream>>>(B_wsp, B_bsp, y2, z1, u);
    u16* xo = layer == 0 ? xb : out;
    k_res_gemm<FH_><<<32 * 8, 256, SM_RES, stream>>>(u, B_wout, xa, xo, layer == 0 ? stB : nullptr);
    xcur = xb;
    stcur = stB;
  }
}
```

```cpp
#include <hip/hip_runtime.h>
#include <stdint.h>

typedef unsigned short u16;
typedef __attribute__((ext_vector_type(8))) short s16x8;
typedef __attribute__((ext_vector_type(4))) short s16x4;
typedef __attribute__((ext_vector_type(4))) float f32x4;
typedef __attribute__((ext_vector_type(16))) float f32x16;
typedef __attribute__((address_space(3))) s16x4* lds_s16x4_ptr;

#define STAMP(k)

constexpr int S_ = 2048, D_ = 640, H_ = 32, DH_ = 20, D3_ = 1920, FH_ = 768, F2_ = 1536;

__device__ __forceinline__ float bf2f(u16 v) { return __uint_as_float(((unsigned)v) << 16); }
__device__ __forceinline__ u16 f2bf(float f) { return __builtin_bit_cast(u16, (__bf16)f); }
__device__ __forceinline__ float rbf(float f) { return bf2f(f2bf(f)); }
__device__ __forceinline__ unsigned pack2(float a, float b) { return (unsigned)f2bf(a) | ((unsigned)f2bf(b) << 16); }
__device__ __forceinline__ float lo_f(unsigned w) { return __uint_as_float(w << 16); }
__device__ __forceinline__ float hi_f(unsigned w) { return __uint_as_float(w & 0xffff0000u); }

__device__ __forceinline__ f32x4 mfma16(s16x8 a, s16x8 b, f32x4 c) {
  return __builtin_amdgcn_mfma_f32_16x16x32_bf16(a, b, c, 0, 0, 0);
}
__device__ __forceinline__ f32x16 mfma32(s16x8 a, s16x8 b, f32x16 c) {
  return __builtin_amdgcn_mfma_f32_32x32x16_bf16(a, b, c, 0, 0, 0);
}
__device__ __forceinline__ s16x4 tr_read(const char* p) {
  return __builtin_amdgcn_ds_read_tr16_b64_v4i16((lds_s16x4_ptr)(uintptr_t)(unsigned)(uintptr_t)p);
}
__device__ __forceinline__ s16x8 cat8(s16x4 a, s16x4 b) {
  s16x8 r;
  r[0] = a[0]; r[1] = a[1]; r[2] = a[2]; r[3] = a[3];
  r[4] = b[0]; r[5] = b[1]; r[6] = b[2]; r[7] = b[3];
  return r;
}

template <int RT, int CT, int R, int C>
__device__ __forceinline__ void tile_map(int b, int& rt, int& ct) {
  static_assert(R * C * 8 == RT * CT && (RT / R) * (CT / C) == 8, "bad xcd map");
  int x = b & 7, idx = b >> 3;
  rt = (x / (CT / C)) * R + idx / C;
  ct = (x % (CT / C)) * C + idx % C;
}

__device__ __forceinline__ void ln_finish(float mean, float var, float& m_b, float& inv) {
  m_b = rbf(mean);
  float v_b = rbf(var);
  float den = rbf(sqrtf(rbf(v_b + 1e-5f)));
  inv = 1.0f / den;
}
template <int CNT>
__device__ __forceinline__ void ln_combine8(float2 part, float& m_b, float& inv) {
  float s = part.x;
  s += __shfl_xor(s, 1); s += __shfl_xor(s, 2); s += __shfl_xor(s, 4);
  float mu = s * 0.125f;
  float dm = part.x - mu;
  float m2 = part.y + (float)CNT * dm * dm;
  m2 += __shfl_xor(m2, 1); m2 += __shfl_xor(m2, 2); m2 += __shfl_xor(m2, 4);
  ln_finish(mu, m2 * (1.0f / (8 * CNT)), m_b, inv);
}

__device__ __forceinline__ uint4 ln_chunk(uint4 raw, float sc, float sh) {
  uint4 r;
  unsigned* s = (unsigned*)&raw;
  unsigned* d = (unsigned*)&r;
#pragma unroll
  for (int i = 0; i < 4; ++i) d[i] = pack2(fmaf(lo_f(s[i]), sc, sh), fmaf(hi_f(s[i]), sc, sh));
  return r;
}

template <int BM, int BN, int BK, int NTH>
struct GemmCfg {
  static constexpr int PSTR = BK * 64;
  static constexpr int NW = NTH / 64;
  static constexpr int NP0 = (BN + 31) / 32;
  static constexpr int NPANEL = ((NP0 * PSTR / 1024) % NW == 0) ? NP0 : NP0 + 1;
  static constexpr int A_BYTES = BM * BK * 2;
  static constexpr int B_BYTES = NPANEL * PSTR;
  static constexpr int STAGE = A_BYTES + B_BYTES;
  static constexpr int A_INS = A_BYTES / 1024 / NW;
  static constexpr int B_INS = B_BYTES / 1024 / NW;
  static constexpr int NS = 3;
  static_assert(A_INS * 1024 * NW == A_BYTES && B_INS * 1024 * NW == B_BYTES, "DMA tiling must divide evenly");
};

__device__ __forceinline__ void dma16(const void* g, char* l) {
  __builtin_amdgcn_global_load_lds((const unsigned*)g, (__attribute__((address_space(3))) unsigned*)(uintptr_t)(unsigned)(uintptr_t)l, 16, 0, 0);
}

template <int BM, int BN, int BK, int NTH, int WGM, int WGN, int KTOT, class BCol>
__device__ __forceinline__ void mainloop(char* smem, const u16* __restrict__ A, int lda, const u16* __restrict__ B, int ldb,
                                         BCol bcol, f32x4 (&acc)[BM / WGM / 16][BN / WGN / 16]) {
  using C = GemmCfg<BM, BN, BK, NTH>;
  constexpr int MT = BM / WGM / 16, NT = BN / WGN / 16, NIT = KTOT / BK;
  constexpr int CA = BK / 8;
  const int tid = threadIdx.x, lane = tid & 63, wid = tid >> 6;
  const int wm = wid / WGN, wn = wid % WGN;
  const int g = lane >> 4, c16 = lane & 15;

  int aoff[C::A_INS], boff[C::B_INS];
#pragma unroll
  for (int i = 0; i < C::A_INS; ++i) {
    int pos = (wid * C::A_INS + i) * 64 + lane;
    int row = pos / CA, cp = pos % CA;
    aoff[i] = row * lda + ((cp ^ (row & 7)) << 3);
  }
#pragma unroll
  for (int i = 0; i < C::B_INS; ++i) {
    int pos = (wid * C::B_INS + i) * 64 + lane;
    int panel = pos / (BK * 4), rem = pos % (BK * 4), kr = rem >> 2, cp = rem & 3;
    int c = panel * 4 + (cp ^ (((kr >> 3) & 1) << 1));
    boff[i] = kr * ldb + bcol(c);
  }
  auto issue = [&](int it) {
    char* buf = smem + (it % C::NS) * C::STAGE;
#pragma unroll
    for (int i = 0; i < C::A_INS; ++i) dma16(A + aoff[i] + it * BK, buf + (wid * C::A_INS + i) * 1024);
#pragma unroll
    for (int i = 0; i < C::B_INS; ++i)
      dma16(B + boff[i] + (size_t)it * BK * ldb, buf + C::A_BYTES + (wid * C::B_INS + i) * 1024);
  };

#pragma unroll
  for (int m = 0; m < MT; ++m)
#pragma unroll
    for (int n = 0; n < NT; ++n) acc[m][n] = f32x4{0.f, 0.f, 0.f, 0.f};

#pragma unroll
  for (int i = 0; i < C::NS - 1; ++i) issue(i < NIT ? i : NIT - 1);
  const int a_off = (wm * MT * 16 + c16) * (BK * 2);
  const int q = c16 >> 2, p4 = lane & 3;
  const int b_off = C::A_BYTES + (8 * g + q) * 64 + p4 * 8;

  for (int it = 0; it < NIT; ++it) {
    {
      constexpr int VM = (C::NS - 2) * (C::A_INS + C::B_INS);
      static_assert(VM < 64, "vmcnt range");
      __builtin_amdgcn_s_waitcnt((VM & 15) | ((VM >> 4) << 14) | (7 << 4) | (15 << 8));
    }
    __builtin_amdgcn_s_barrier();
    issue(it + C::NS - 1 < NIT ? it + C::NS - 1 : NIT - 1);
    const char* buf = smem + (it % C::NS) * C::STAGE;
#pragma unroll
    for (int ks = 0; ks < BK / 32; ++ks) {
      s16x8 af[MT], bfr[NT];
#pragma unroll
      for (int m = 0; m < MT; ++m)
        af[m] = *(const s16x8*)(buf + a_off + m * 16 * (BK * 2) + (((4 * ks + g) ^ (c16 & 7)) << 4));
#pragma unroll
      for (int n = 0; n < NT; ++n) {
        int nb = wn * NT + n;
        const char* p = buf + b_off + (nb >> 1) * C::PSTR + ks * 32 * 64 + (((nb & 1) ^ (g & 1)) << 5);
        bfr[n] = cat8(tr_read(p), tr_read(p + 4 * 64));
      }
#pragma unroll
      for (int m = 0; m < MT; ++m)
#pragma unroll
        for (int n = 0; n < NT; ++n) acc[m][n] = mfma16(bfr[n], af[m], acc[m][n]);
    }
  }
  asm volatile("s_waitcnt vmcnt(0)" ::: "memory");
  __syncthreads();
}

template <int MT, int NT, int STR>
__device__ __forceinline__ void acc_to_lds(u16* T, const f32x4 (&acc)[MT][NT], int row0, int col0) {
  const int lane = threadIdx.x & 63, g = lane >> 4, c16 = lane & 15;
#pragma unroll
  for (int m = 0; m < MT; ++m)
#pragma unroll
    for (int n = 0; n < NT; ++n)
      *(uint2*)(T + (row0 + m * 16 + c16) * STR + col0 + n * 16 + 4 * g) =
          make_uint2(pack2(acc[m][n][0], acc[m][n][1]), pack2(acc[m][n][2], acc[m][n][3]));
}

__device__ __forceinline__ void chunk_stats(const float (&y)[8], float& mu, float& m2) {
  float s = ((y[0] + y[1]) + (y[2] + y[3])) + ((y[4] + y[5]) + (y[6] + y[7]));
  mu = s * 0.125f;
  m2 = 0.f;
#pragma unroll
  for (int e = 0; e < 8; ++e) m2 += (y[e] - mu) * (y[e] - mu);
}

__global__ __launch_bounds__(256) void k_stats0(const u16* __restrict__ x, float2* __restrict__ st,
                                                const u16* __restrict__ wqkv, const u16* __restrict__ win,
                                                float* __restrict__ csq, float* __restrict__ csw) {
  if (blockIdx.x >= S_ / 4) {
    __shared__ float red[4][64];
    int cb = blockIdx.x - S_ / 4;
    const u16* w = cb < 30 ? wqkv : win;
    int n = cb < 30 ? D3_ : F2_;
    int col = (cb < 30 ? cb : cb - 30) * 64 + (threadIdx.x & 63);
    int rg = threadIdx.x >> 6;
    float acc = 0.f;
#pragma unroll 16
    for (int r = rg * 160; r < rg * 160 + 160; ++r) acc += bf2f(w[(size_t)r * n + col]);
    red[rg][threadIdx.x & 63] = acc;
    __syncthreads();
    if (threadIdx.x < 64) (cb < 30 ? csq : csw)[col] = (red[0][threadIdx.x] + red[1][threadIdx.x]) + (red[2][threadIdx.x] + red[3][threadIdx.x]);
    return;
  }
  int row = blockIdx.x * 4 + (threadIdx.x >> 6), lane = threadIdx.x & 63;
  const unsigned* p = (const unsigned*)(x + (size_t)row * D_ + lane * 10);
  float v[10];
#pragma unroll
  for (int i = 0; i < 5; ++i) {
    unsigned w = p[i];
    v[2 * i] = lo_f(w);
    v[2 * i + 1] = hi_f(w);
  }
  float s = 0.f;
#pragma unroll
  for (int i = 0; i < 10; ++i) s += v[i];
  s += __shfl_xor(s, 1); s += __shfl_xor(s, 2); s += __shfl_xor(s, 4);
  float mu = s * (1.0f / 80.0f), m2 = 0.f;
#pragma unroll
  for (int i = 0; i < 10; ++i) m2 += (v[i] - mu) * (v[i] - mu);
  m2 += __shfl_xor(m2, 1); m2 += __shfl_xor(m2, 2); m2 += __shfl_xor(m2, 4);
  if ((lane & 7) == 0) st[row * 8 + (lane >> 3)] = make_float2(mu, m2);
}

__device__ __forceinline__ float2 row_ln(const float2* __restrict__ st, int row) {
  const float4* p = (const float4*)(st + (size_t)row * 8);
  float4 a = p[0], b = p[1], c = p[2], d = p[3];
  float mu = ((a.x + a.z) + (b.x + b.z) + (c.x + c.z) + (d.x + d.z)) * 0.125f;
  float m2 = (a.y + a.w) + (b.y + b.w) + (c.y + c.w) + (d.y + d.w);
  float e;
  e = a.x - mu; m2 += 80.f * e * e; e = a.z - mu; m2 += 80.f * e * e;
  e = b.x - mu; m2 += 80.f * e * e; e = b.z - mu; m2 += 80.f * e * e;
  e = c.x - mu; m2 += 80.f * e * e; e = c.z - mu; m2 += 80.f * e * e;
  e = d.x - mu; m2 += 80.f * e * e; e = d.z - mu; m2 += 80.f * e * e;
  float m_b, inv;
  ln_finish(mu, m2 * (1.0f / 640.0f), m_b, inv);
  return make_float2(inv, m_b * inv);
}

constexpr int TSTR = 176;
constexpr int PSTRIDE = 168;
constexpr int KVP = 24;
__global__ __launch_bounds__(512) void k_qkv(const u16* __restrict__ x, const float2* __restrict__ st,
                                             const u16* __restrict__ wqkv, const u16* __restrict__ wphi,
                                             u16* __restrict__ phiq, float* __restrict__ kvpart, const float* __restrict__ csq) {
  extern __shared__ __attribute__((aligned(16))) char smem[];
  constexpr int BM = 128, BN = 160, BK = 64, NTH = 512;
  using C = GemmCfg<BM, BN, BK, NTH>;
  const int tid = threadIdx.x, lane = tid & 63, wid = tid >> 6;
  int rt, ct;
  tile_map<16, 12, 4, 6>(blockIdx.x, rt, ct);
  const bool isq = ct < 4;
  const int row0 = rt * BM;

  auto bcol = [&](int c) { return c >= 20 ? 0 : (isq ? ct * 160 + c * 8 : (c < 10 ? D_ + (ct - 4) * 80 + c * 8 : 2 * D_ + (ct - 4) * 80 + (c - 10) * 8)); };
  float2 lnr[2];
#pragma unroll
  for (int m = 0; m < 2; ++m) lnr[m] = row_ln(st, row0 + (wid >> 1) * 32 + m * 16 + (lane & 15));
  const int hh = lane >> 5, c32 = lane & 31;
  const int j = isq ? wid : (wid & 3);
  const int h = isq ? ct * 8 + j : (ct - 4) * 4 + j;

  f32x4 acc[2][5];
  mainloop<BM, BN, BK, NTH, 4, 2, D_>(smem, x + (size_t)row0 * D_, D_, wqkv, D3_, bcol, acc);
  {
#pragma unroll
    for (int n = 0; n < 5; ++n) {
      int cc = (wid & 1) * 10 + n * 2 + ((lane >> 4) >> 1);
      f32x4 cs = *(const f32x4*)(csq + bcol(cc) + 4 * ((lane >> 4) & 1));
#pragma unroll
      for (int m = 0; m < 2; ++m)
#pragma unroll
        for (int i = 0; i < 4; ++i) acc[m][n][i] = fmaf(acc[m][n][i], lnr[m].x, -lnr[m].y * cs[i]);
    }
  }
  s16x8 wA[2];
#pragma unroll
  for (int ks = 0; ks < 2; ++ks) {
#pragma unroll
    for (int e = 0; e < 8; ++e) {
      int d = 16 * ks + 8 * hh + e;
      wA[ks][e] = (c32 < DH_ && d < DH_) ? (short)wphi[(h * DH_ + d) * DH_ + c32] : (short)0;
    }
  }

  u16* T = (u16*)smem;
  u16* P = (u16*)(smem + 128 * TSTR * 2);
  acc_to_lds<2, 5, TSTR>(T, acc, (wid >> 1) * 32, (wid & 1) * 80);
  __syncthreads();
  {
    const int nmt = isq ? 4 : 2;
#pragma unroll 2
    for (int mi = 0; mi < 4; ++mi) {
      if (mi < nmt) {
        const int mt = isq ? mi : 2 * (wid >> 2) + mi;
        const u16* src = T + (mt * 32 + c32) * TSTR + 20 * j + 8 * hh;
        s16x8 b0 = cat8(*(const s16x4*)src, *(const s16x4*)(src + 4));
        s16x8 b1 = cat8(*(const s16x4*)(src + 16), *(const s16x4*)(src + 20));
        f32x16 pp;
#pragma unroll
        for (int r = 0; r < 16; ++r) pp[r] = 0.f;
        pp = mfma32(wA[0], b0, pp);
        pp = mfma32(wA[1], b1, pp);
        float a[12];
#pragma unroll
        for (int r = 0; r < 12; ++r) a[r] = rbf(pp[r]);
        float mx = a[0], mn = a[0];
#pragma unroll
        for (int r = 1; r < 8; ++r) { mx = fmaxf(mx, a[r]); mn = fminf(mn, a[r]); }
        if (hh == 0) {
#pragma unroll
          for (int r = 8; r < 12; ++r) { mx = fmaxf(mx, a[r]); mn = fminf(mn, a[r]); }
        }
        mx = fmaxf(mx, __shfl_xor(mx, 32));
        mn = fminf(mn, __shfl_xor(mn, 32));
        float e1[12], e2[12], s1 = 0.f, s2 = 0.f;
#pragma unroll
        for (int r = 0; r < 12; ++r) {
          e1[r] = __expf(a[r] - mx);
          e2[r] = __expf(mn - a[r]);
          if (r < 8 || hh == 0) { s1 += e1[r]; s2 += e2[r]; }
        }
        s1 += __shfl_xor(s1, 32);
        s2 += __shfl_xor(s2, 32);
        float r1 = 1.0f / s1, r2 = 1.0f / s2;
        u16* dst = isq ? phiq + (size_t)(row0 + mt * 32 + c32) * 1280 + h * 40 : P + (mt * 32 + c32) * PSTRIDE + 40 * j;
#pragma unroll
        for (int qd = 0; qd < 3; ++qd) {
          if (qd < 2 || hh == 0) {
            int f0 = 8 * qd + 4 * hh;
            *(uint2*)(dst + f0) = make_uint2(pack2(e1[4 * qd] * r1, e1[4 * qd + 1] * r1),
                                             pack2(e1[4 * qd + 2] * r1, e1[4 * qd + 3] * r1));
            *(uint2*)(dst + 20 + f0) = make_uint2(pack2(e2[4 * qd] * r2, e2[4 * qd + 1] * r2),
                                                  pack2(e2[4 * qd + 2] * r2, e2[4 * qd + 3] * r2));
          }
        }
      }
    }
  }
  if (isq) return;
  __syncthreads();
  if (wid >= 4) return;
  {
    const int g = lane >> 4, c16 = lane & 15;
    const int q = c16 >> 2, p4 = lane & 3;
    f32x4 kacc[3][2];
#pragma unroll
    for (int a = 0; a < 3; ++a)
#pragma unroll
      for (int b = 0; b < 2; ++b) kacc[a][b] = f32x4{0.f, 0.f, 0.f, 0.f};
    s16x8 ones;
#pragma unroll
    for (int e = 0; e < 8; ++e) ones[e] = (c16 == 4) ? (short)0x3F80 : (short)0;
#pragma unroll
    for (int ks = 0; ks < 4; ++ks) {
      int r0 = 32 * ks + 8 * g + q;
      s16x8 af[3], bfr[2];
#pragma unroll
      for (int ft = 0; ft < 3; ++ft) {
        const char* p = (const char*)(P + r0 * PSTRIDE + 40 * j + 16 * ft + 4 * p4);
        af[ft] = cat8(tr_read(p), tr_read(p + 4 * PSTRIDE * 2));
      }
#pragma unroll
      for (int nt = 0; nt < 2; ++nt) {
        const char* p = (const char*)(T + r0 * TSTR + 80 + 20 * j + 16 * nt + 4 * p4);
        bfr[nt] = cat8(tr_read(p), tr_read(p + 4 * TSTR * 2));
      }
      if (c16 >= 4) bfr[1] = ones;
#pragma unroll
      for (int ft = 0; ft < 3; ++ft)
#pragma unroll
        for (int nt = 0; nt < 2; ++nt) kacc[ft][nt] = mfma16(af[ft], bfr[nt], kacc[ft][nt]);
    }
    float* dst = kvpart + (size_t)(rt * H_ + h) * 40 * KVP;
#pragma unroll
    for (int ft = 0; ft < 3; ++ft)
#pragma unroll
      for (int nt = 0; nt < 2; ++nt)
#pragma unroll
        for (int i = 0; i < 4; ++i) {
          int f = 16 * ft + 4 * g + i, d = 16 * nt + c16;
          if (f < 40 && d <= 20) dst[f * KVP + d] = kacc[ft][nt][i];
        }
  }
}

__global__ __launch_bounds__(256) void k_o(const float* __restrict__ kvpart, const u16* __restrict__ phiq,
                                           u16* __restrict__ o) {
  __shared__ __attribute__((aligned(16))) u16 KV[32 * 64];
  const int tid = threadIdx.x, lane = tid & 63, wid = tid >> 6;
  const int h = blockIdx.x >> 3, tt = blockIdx.x & 7;
  const int g = lane >> 4, c16 = lane & 15;
  s16x8 b0[4], b1[4];
#pragma unroll
  for (int nt = 0; nt < 4; ++nt) {
    int tok = tt * 256 + wid * 64 + nt * 16 + c16;
    const u16* src = phiq + (size_t)tok * 1280 + h * 40 + 8 * g;
    b0[nt] = *(const s16x8*)src;
    if (g == 0) b1[nt] = *(const s16x8*)(src + 32);
    else {
#pragma unroll
      for (int e = 0; e < 8; ++e) b1[nt][e] = 0;
    }
  }
  float s[4];
#pragma unroll
  for (int i = 0; i < 4; ++i) {
    int e = tid + 256 * i;
    s[i] = 0.f;
    if (e < 40 * KVP) {
      const float* p = kvpart + (size_t)h * 40 * KVP + e;
#pragma unroll
      for (int r = 0; r < 16; ++r) s[i] += p[(size_t)r * H_ * 40 * KVP];
    }
  }
  for (int e = tid; e < 32 * 64; e += 256) KV[e] = 0;
  __syncthreads();
#pragma unroll
  for (int i = 0; i < 4; ++i) {
    int e = tid + 256 * i;
    int f = e / KVP, d = e % KVP;
    if (e < 40 * KVP && d <= 20) KV[d * 64 + f] = f2bf(s[i]);
  }
  __syncthreads();
  s16x8 af[2][2];
#pragma unroll
  for (int mt = 0; mt < 2; ++mt)
#pragma unroll
    for (int ks = 0; ks < 2; ++ks) af[mt][ks] = *(const s16x8*)(KV + (16 * mt + c16) * 64 + 32 * ks + 8 * g);
#pragma unroll
  for (int nt = 0; nt < 4; ++nt) {
    int tok = tt * 256 + wid * 64 + nt * 16 + c16;
    f32x4 z4 = f32x4{0.f, 0.f, 0.f, 0.f};
    f32x4 c0 = mfma16(af[0][0], b0[nt], z4);
    c0 = mfma16(af[0][1], b1[nt], c0);
    f32x4 c1 = mfma16(af[1][0], b0[nt], z4);
    c1 = mfma16(af[1][1], b1[nt], c1);
    float den = __shfl(c1[0], 16 + c16);
    den = rbf(rbf(den) + 1e-6f);
    u16* dst = o + (size_t)tok * D_ + h * DH_;
    *(uint2*)(dst + 4 * g) = make_uint2(pack2(rbf(c0[0]) / den, rbf(c0[1]) / den), pack2(rbf(c0[2]) / den, rbf(c0[3]) / den));
    if (g == 0)
      *(uint2*)(dst + 16) = make_uint2(pack2(rbf(c1[0]) / den, rbf(c1[1]) / den), pack2(rbf(c1[2]) / den, rbf(c1[3]) / den));
  }
}

template <int KTOT>
__global__ __launch_bounds__(256) void k_res_gemm(const u16* __restrict__ a, const u16* __restrict__ w,
                                                  const u16* __restrict__ xin, u16* __restrict__ xo,
                                                  float2* __restrict__ st) {
  extern __shared__ __attribute__((aligned(16))) char smem[];
  constexpr int BM = 64, BN = 80, BK = 128, NTH = 256, STR = 88;
  using C = GemmCfg<BM, BN, BK, NTH>;
  const int tid = threadIdx.x, lane = tid & 63, wid = tid >> 6;
  int rt, ct;
  tile_map<32, 8, 8, 4>(blockIdx.x, rt, ct);
  const int row0 = rt * BM, col0 = ct * BN;
  uint4 xr[3];
#pragma unroll
  for (int i = 0; i < 3; ++i) {
    int id = tid + 256 * i;
    if (id < 640) xr[i] = *(const uint4*)(xin + (size_t)(row0 + id / 10) * D_ + col0 + (id % 10) * 8);
  }
  auto bcol = [&](int c) { return c < 10 ? col0 + c * 8 : col0; };
  f32x4 acc[1][5];
  mainloop<BM, BN, BK, NTH, 4, 1, KTOT>(smem, a + (size_t)row0 * KTOT, KTOT, w, D_, bcol, acc);
  u16* T = (u16*)smem;
  float2* cs = (float2*)(smem + BM * STR * 2);
  acc_to_lds<1, 5, STR>(T, acc, wid * 16, 0);
  __syncthreads();
#pragma unroll
  for (int i = 0; i < 3; ++i) {
    int id = tid + 256 * i;
    if (id < 640) {
      int r = id / 10, c = id % 10;
      uint4 tv = *(const uint4*)(T + r * STR + c * 8);
      const unsigned* tp = (const unsigned*)&tv;
      const unsigned* xp = (const unsigned*)&xr[i];
      float y[8];
#pragma unroll
      for (int e = 0; e < 4; ++e) {
        y[2 * e] = rbf(lo_f(xp[e]) + lo_f(tp[e]));
        y[2 * e + 1] = rbf(hi_f(xp[e]) + hi_f(tp[e]));
      }
      uint4 ov = make_uint4(pack2(y[0], y[1]), pack2(y[2], y[3]), pack2(y[4], y[5]), pack2(y[6], y[7]));
      *(uint4*)(xo + (size_t)(row0 + r) * D_ + col0 + c * 8) = ov;
      float mu, m2;
      chunk_stats(y, mu, m2);
      cs[r * 10 + c] = make_float2(mu, m2);
    }
  }
  if (st != nullptr) {
    __syncthreads();
    if (tid < BM) {
      float mu = 0.f;
#pragma unroll
      for (int c = 0; c < 10; ++c) mu += cs[tid * 10 + c].x;
      mu *= 0.1f;
      float m2 = 0.f;
#pragma unroll
      for (int c = 0; c < 10; ++c) {
        float2 v = cs[tid * 10 + c];
        m2 += v.y + 8.0f * (v.x - mu) * (v.x - mu);
      }
      st[(row0 + tid) * 8 + ct] = make_float2(mu, m2);
    }
  }
}

__device__ __forceinline__ float gelu_tanh(float y) {
  float u = 0.7978845608028654f * (y + 0.044715f * y * y * y);
  float e = __expf(2.0f * u);
  float th = 1.0f - 2.0f / (e + 1.0f);
  return 0.5f * y * (1.0f + th);
}
__global__ __launch_bounds__(512) void k_win(const u16* __restrict__ x, const float2* __restrict__ st,
                                             const u16* __restrict__ win, u16* __restrict__ z1,
                                             u16* __restrict__ y2, float2* __restrict__ yst, const float* __restrict__ csw) {
  extern __shared__ __attribute__((aligned(16))) char smem[];
  constexpr int BM = 128, BN = 96, BK = 64, NTH = 512, STR = 104;
  using C = GemmCfg<BM, BN, BK, NTH>;
  const int tid = threadIdx.x, lane = tid & 63, wid = tid >> 6;
  int rt, ct;
  tile_map<16, 16, 4, 8>(blockIdx.x, rt, ct);
  const int row0 = rt * BM, col0 = ct * BN;
  float2 lnr[2];
#pragma unroll
  for (int m = 0; m < 2; ++m) lnr[m] = row_ln(st, row0 + (wid >> 1) * 32 + m * 16 + (lane & 15));
  auto bcol = [&](int c) { return c < 12 ? col0 + c * 8 : col0; };
  f32x4 acc[2][3];
  mainloop<BM, BN, BK, NTH, 4, 2, D_>(smem, x + (size_t)row0 * D_, D_, win, F2_, bcol, acc);
#pragma unroll
  for (int n = 0; n < 3; ++n) {
    f32x4 cs = *(const f32x4*)(csw + col0 + (wid & 1) * 48 + n * 16 + 4 * (lane >> 4));
#pragma unroll
    for (int m = 0; m < 2; ++m)
#pragma unroll
      for (int i = 0; i < 4; ++i) acc[m][n][i] = fmaf(acc[m][n][i], lnr[m].x, -lnr[m].y * cs[i]);
  }
  const bool second = ct >= 8;
  u16* dst = second ? y2 : z1;
  const int cbase = second ? col0 - FH_ : col0;
#pragma unroll
  for (int m = 0; m < 2; ++m)
#pragma unroll
    for (int n = 0; n < 3; ++n)
#pragma unroll
      for (int i = 0; i < 4; ++i) acc[m][n][i] = gelu_tanh(rbf(acc[m][n][i]));
  u16* T = (u16*)smem;
  float2* cs = (float2*)(smem + BM * STR * 2);
  acc_to_lds<2, 3, STR>(T, acc, (wid >> 1) * 32, (wid & 1) * 48);
  __syncthreads();
#pragma unroll
  for (int i = 0; i < 3; ++i) {
    int id = tid + NTH * i, r = id / 12, c = id % 12;
    uint4 tv = *(const uint4*)(T + r * STR + c * 8);
    *(uint4*)(dst + (size_t)(row0 + r) * FH_ + cbase + c * 8) = tv;
    if (second) {
      const unsigned* tp = (const unsigned*)&tv;
      float y[8];
#pragma unroll
      for (int e = 0; e < 4; ++e) { y[2 * e] = lo_f(tp[e]); y[2 * e + 1] = hi_f(tp[e]); }
      float mu, m2;
      chunk_stats(y, mu, m2);
      cs[id] = make_float2(mu, m2);
    }
  }
  if (second) {
    __syncthreads();
    if (tid < BM) {
      float mu = 0.f;
#pragma unroll
      for (int c = 0; c < 12; ++c) mu += cs[tid * 12 + c].x;
      mu *= (1.0f / 12.0f);
      float m2 = 0.f;
#pragma unroll
      for (int c = 0; c < 12; ++c) {
        float2 v = cs[tid * 12 + c];
        m2 += v.y + 8.0f * (v.x - mu) * (v.x - mu);
      }
      yst[(row0 + tid) * 8 + (ct - 8)] = make_float2(mu, m2);
    }
  }
}

__global__ __launch_bounds__(256) void k_norm(u16* __restrict__ y2, const float2* __restrict__ yst) {
  int row = blockIdx.x * 4 + (threadIdx.x >> 6), lane = threadIdx.x & 63;
  u16* p = y2 + (size_t)row * FH_ + lane * 12;
  uint2 a = *(const uint2*)p, b = *(const uint2*)(p + 4), c = *(const uint2*)(p + 8);
  float m_b, inv;
  ln_combine8<96>(yst[row * 8 + (lane & 7)], m_b, inv);
  auto f = [&](unsigned w) { return pack2(rbf(lo_f(w) - m_b) * inv, rbf(hi_f(w) - m_b) * inv); };
  *(uint2*)p = make_uint2(f(a.x), f(a.y));
  *(uint2*)(p + 4) = make_uint2(f(b.x), f(b.y));
  *(uint2*)(p + 8) = make_uint2(f(c.x), f(c.y));
}

__global__ __launch_bounds__(512) void k_spatial(const u16* __restrict__ wsp, const u16* __restrict__ bsp,
                                                 const u16* __restrict__ y2,
                                                 const u16* __restrict__ z1, u16* __restrict__ u) {
  extern __shared__ __attribute__((aligned(16))) char smem[];
  constexpr int BM = 64, BN = 96, BK = 128, NTH = 512, STR = 104;
  using C = GemmCfg<BM, BN, BK, NTH>;
  const int tid = threadIdx.x, lane = tid & 63, wid = tid >> 6;
  int rt, ct;
#define DBG_SLOT 0
  STAMP(0);
  tile_map<32, 8, 8, 4>(blockIdx.x, rt, ct);
  const int row0 = rt * BM, col0 = ct * BN;
  auto bcol = [&](int c) { return col0 + c * 8; };
  f32x4 acc[1][3];
  STAMP(1);
  mainloop<BM, BN, BK, NTH, 4, 2, S_>(smem, wsp + (size_t)row0 * S_, S_, y2, FH_, bcol, acc);
  STAMP(2);
  u16* T = (u16*)smem;
  acc_to_lds<1, 3, STR>(T, acc, (wid >> 1) * 16, (wid & 1) * 48);
  __syncthreads();
#pragma unroll
  for (int i = 0; i < 2; ++i) {
    int id = tid + NTH * i, r = id / 12, c = id % 12;
    if (id < BM * 12) {
      uint4 tv = *(const uint4*)(T + r * STR + c * 8);
      size_t idx = (size_t)(row0 + r) * FH_ + col0 + c * 8;
      uint4 zv = *(const uint4*)(z1 + idx);
      float b = bf2f(bsp[row0 + r]);
      const unsigned* tp = (const unsigned*)&tv;
      const unsigned* zp = (const unsigned*)&zv;
      unsigned ov[4];
#pragma unroll
      for (int e = 0; e < 4; ++e)
        ov[e] = pack2(lo_f(zp[e]) * rbf(lo_f(tp[e]) + b), hi_f(zp[e]) * rbf(hi_f(tp[e]) + b));
      *(uint4*)(u + idx) = make_uint4(ov[0], ov[1], ov[2], ov[3]);
    }
  }
  STAMP(3);
#undef DBG_SLOT
}

extern "C" void kernel_launch(void* const* d_in, const int* in_sizes, int n_in, void* d_out, int out_size,
                              void* d_ws, size_t ws_size, hipStream_t stream) {
  const u16* A_x = (const u16*)d_in[0];
  const u16* A_wqkv = (const u16*)d_in[1];
  const u16* A_wphi = (const u16*)d_in[2];
  const u16* A_wo = (const u16*)d_in[3];
  const u16* B_win = (const u16*)d_in[5];
  const u16* B_wsp = (const u16*)d_in[6];
  const u16* B_bsp = (const u16*)d_in[7];
  const u16* B_wout = (const u16*)d_in[8];
  u16* out = (u16*)d_out;

  char* ws = (char*)d_ws;
  size_t off = 0;
  auto alloc = [&](size_t bytes) { char* p = ws + off; off += (bytes + 255) & ~(size_t)255; return p; };
  u16* xa = (u16*)alloc((size_t)S_ * D_ * 2);
  u16* xb = (u16*)alloc((size_t)S_ * D_ * 2);
  float2* st0 = (float2*)alloc((size_t)S_ * 8 * 8);
  float2* stA = (float2*)alloc((size_t)S_ * 8 * 8);
  float2* stB = (float2*)alloc((size_t)S_ * 8 * 8);
  float2* yst = (float2*)alloc((size_t)S_ * 8 * 8);
  u16* phiq = (u16*)alloc((size_t)S_ * 1280 * 2);
  float* kvpart = (float*)alloc((size_t)16 * H_ * 40 * KVP * 4);
  u16* o = (u16*)alloc((size_t)S_ * D_ * 2);
  u16* z1 = (u16*)alloc((size_t)S_ * FH_ * 2);
  u16* y2 = (u16*)alloc((size_t)S_ * FH_ * 2);
  u16* u = (u16*)alloc((size_t)S_ * FH_ * 2);
  float* csq = (float*)alloc(D3_ * 4);
  float* csw = (float*)alloc(F2_ * 4);

  constexpr int SM_QKV = (GemmCfg<128, 160, 64, 512>::NS * GemmCfg<128, 160, 64, 512>::STAGE > 128 * (TSTR + PSTRIDE) * 2)
                             ? GemmCfg<128, 160, 64, 512>::NS * GemmCfg<128, 160, 64, 512>::STAGE
                             : 128 * (TSTR + PSTRIDE) * 2;
  constexpr int SM_RES = GemmCfg<64, 80, 128, 256>::NS * GemmCfg<64, 80, 128, 256>::STAGE;
  constexpr int SM_WIN = GemmCfg<128, 96, 64, 512>::NS * GemmCfg<128, 96, 64, 512>::STAGE;
  constexpr int SM_SP = GemmCfg<64, 96, 128, 512>::NS * GemmCfg<64, 96, 128, 512>::STAGE;
  hipFuncSetAttribute((const void*)k_qkv, hipFuncAttributeMaxDynamicSharedMemorySize, SM_QKV);
  hipFuncSetAttribute((const void*)k_res_gemm<D_>, hipFuncAttributeMaxDynamicSharedMemorySize, SM_RES);
  hipFuncSetAttribute((const void*)k_res_gemm<FH_>, hipFuncAttributeMaxDynamicSharedMemorySize, SM_RES);
  hipFuncSetAttribute((const void*)k_win, hipFuncAttributeMaxDynamicSharedMemorySize, SM_WIN);
  hipFuncSetAttribute((const void*)k_spatial, hipFuncAttributeMaxDynamicSharedMemorySize, SM_SP);

  k_stats0<<<S_ / 4 + 30 + 24, 256, 0, stream>>>(A_x, st0, A_wqkv, B_win, csq, csw);
  const u16* xcur = A_x;
  const float2* stcur = st0;
  for (int layer = 0; layer < 2; ++layer) {
    k_qkv<<<16 * 12, 512, SM_QKV, stream>>>(xcur, stcur, A_wqkv, A_wphi, phiq, kvpart, csq);
    k_o<<<32 * 8, 256, 0, stream>>>(kvpart, phiq, o);
    k_res_gemm<D_><<<32 * 8, 256, SM_RES, stream>>>(o, A_wo, xcur, xa, stA);
    k_win<<<16 * 16, 512, SM_WIN, stream>>>(xa, stA, B_win, z1, y2, yst, csw);
    k_norm<<<S_ / 4, 256, 0, stream>>>(y2, yst);
    k_spatial<<<32 * 8, 512, SM_SP, stream>>>(B_wsp, B_bsp, y2, z1, u);
    u16* xo = layer == 0 ? xb : out;
    k_res_gemm<FH_><<<32 * 8, 256, SM_RES, stream>>>(u, B_wout, xa, xo, layer == 0 ? stB : nullptr);
    xcur = xb;
    stcur = stB;
  }
}
```
